```python
import torch

def _softmax(x, axis=-1):
    return torch.softmax(x, dim=axis)
def _gelu(x):
    return torch.nn.functional.gelu(x, approximate="tanh")
def _silu(x):
    return torch.nn.functional.silu(x)
def _sigmoid(x):
    return torch.sigmoid(x)
def _layernorm(x, axis=-1, eps=1e-5):
    m = x.mean(dim=axis, keepdim=True)
    v = x.var(dim=axis, keepdim=True, unbiased=False)
    return (x - m) / torch.sqrt(v + eps)
def _scatter_add(tgt, idx, src, axis=0):
    return tgt.clone().index_add_(axis, idx, src)
def _segment_sum(src, seg, n):
    out = torch.zeros((n,) + src.shape[1:], dtype=src.dtype)
    return out.index_add_(0, seg, src)
def _pad(x, pw):
    flat = tuple(v for pair in reversed(pw) for v in pair)
    return torch.nn.functional.pad(x, flat)

kernel_name = 'standalone__standard_2d_1d_3d_conv__vision'

def setup_inputs():

    _w = (lambda a: (lambda t: t.to(torch.bfloat16) if t.is_floating_point() else t)(torch.as_tensor(a)))
    H = W = 384
    C = 96
    K = 3
    return {
        "x": _w((lambda *s: torch.randn(*s, dtype=torch.bfloat16) * 0.05)(1, H, W, C)),
        "w": _w((lambda *s: torch.randn(*s, dtype=torch.bfloat16) * 0.05)(K, K, C, C)),
        "b": _w((lambda *s: torch.randn(*s, dtype=torch.bfloat16) * 0.05)(C)),
    }

def reference(x, w, b):
    B, H, W, Cin = x.shape
    K = w.shape[0]
    pl = (K - 1) // 2
    pr = K // 2
    xp = _pad(x, ((0, 0), (pl, pr), (pl, pr), (0, 0)))
    out = sum(
        torch.einsum('bhwc,cd->bhwd', xp[:, i:i+H, j:j+W, :], w[i, j])
        for i in range(K) for j in range(K)
    )
    return out + b[None, None, None, :]
```

```cpp
#include <hip/hip_runtime.h>
#include <hip/hip_bf16.h>
typedef __attribute__((ext_vector_type(8))) short bf16x8;
typedef __attribute__((ext_vector_type(4))) float f32x4;
typedef __attribute__((ext_vector_type(2))) float f32x2;
typedef __attribute__((ext_vector_type(2))) __bf16 bf16x2;
constexpr int H=384,W=384,C=96;
__global__ __launch_bounds__(256) void conv_v0(const unsigned short* __restrict__ x, const unsigned short* __restrict__ w, const unsigned short* __restrict__ b, unsigned short* __restrict__ out){
  __shared__ unsigned short wt[96*104];
  int tid=threadIdx.x, lane=tid&63, wid=tid>>6;
  int p=lane&15, g=lane>>4;
  int bx=blockIdx.x%6, by=blockIdx.x/6;
  int y=by*4+wid, x0=bx*64;
  f32x4 acc[4][6];
  for(int m=0;m<4;m++)for(int n=0;n<6;n++){ for(int j=0;j<4;j++){ unsigned u=((unsigned)b[n*16+g*4+j])<<16; acc[m][n][j]=__builtin_bit_cast(float,u);} }
  for(int t=0;t<9;t++){
    int dy=t/3,dx=t%3;
    __syncthreads();
    for(int i=tid;i<96*96;i+=256){int ci=i/96,co=i%96; wt[co*104+ci]=w[t*9216+i];}
    __syncthreads();
    int yy=y+dy-1;
    for(int kc=0;kc<3;kc++){
      bf16x8 wf[6];
      for(int n=0;n<6;n++) wf[n]=*(const bf16x8*)&wt[(n*16+p)*104+kc*32+g*8];
      for(int m=0;m<4;m++){
        int xx=x0+m*16+p+dx-1;
        bf16x8 xf={0,0,0,0,0,0,0,0};
        if(yy>=0&&yy<H&&xx>=0&&xx<W) xf=*(const bf16x8*)&x[((size_t)yy*W+xx)*C+kc*32+g*8];
        for(int n=0;n<6;n++) acc[m][n]=__builtin_amdgcn_mfma_f32_16x16x32_bf16(wf[n],xf,acc[m][n],0,0,0);
      }
    }
  }
  for(int m=0;m<4;m++)for(int n=0;n<6;n++){
    f32x2 lo={acc[m][n][0],acc[m][n][1]},hi={acc[m][n][2],acc[m][n][3]};
    bf16x2 l=__builtin_convertvector(lo,bf16x2),h=__builtin_convertvector(hi,bf16x2);
    uint2 v; v.x=__builtin_bit_cast(unsigned,l); v.y=__builtin_bit_cast(unsigned,h);
    *(uint2*)&out[((size_t)y*W+x0+m*16+p)*C+n*16+g*4]=v;
  }
}
extern "C" void kernel_launch(void* const* d_in, const int* in_sizes, int n_in, void* d_out, int out_size, void* d_ws, size_t ws_size, hipStream_t stream){
  conv_v0<<<6*96,256,0,stream>>>((const unsigned short*)d_in[0],(const unsigned short*)d_in[1],(const unsigned short*)d_in[2],(unsigned short*)d_out);
}
```

```cpp
#include <hip/hip_runtime.h>
#include <hip/hip_bf16.h>

extern "C" __global__ __launch_bounds__(256, 1) void conv3x3_kernel(const void* x, const void* w, const void* b, void* out) {
  __shared__ __attribute__((aligned(16))) char lds[163840];
  asm volatile(
    "s_load_dwordx8 s[16:23], %0, 0x0\n"
    "v_mbcnt_lo_u32_b32 v0, -1, 0\n"
    "v_mbcnt_hi_u32_b32 v0, -1, v0\n"
    "s_getpc_b64 s[72:73]\n"
    "s_and_b32 s72, s72, 0xfffff000\n"
    "s_mul_i32 s58, %2, 24\n"
    "v_min_u32 v156, 23, v0\n"
    "v_add_lshl_u32 v156, v156, s58, 7\n"
    "global_load_dword v157, v156, s[72:73]\n"
    "s_mul_i32 s58, %2, 98\n"
    "v_add_u32 v1, s58, v0\n"
    "s_cmp_eq_u32 %2, 3\n"
    "s_cselect_b32 s71, 50, 49\n"
    "v_add_u32 v3, s71, v1\n"
    "s_lshl_b32 s71, s71, 4\n"
    "v_mul_u32_u24 v2, 1366, v1\n"
    "s_and_b32 s55, %1, 7\n"
    "s_lshl_b32 s55, s55, 4\n"
    "s_lshr_b32 s56, %1, 4\n"
    "s_add_u32 s55, s55, s56\n"
    "s_mul_i32 s56, s55, 43\n"
    "s_lshr_b32 s56, s56, 9\n"
    "s_mul_i32 s57, s56, 12\n"
    "s_sub_u32 s55, s55, s57\n"
    "s_mul_i32 s70, s56, 36\n"
    "s_add_u32 s70, s70, 18\n"
    "s_bfe_u32 s57, %1, 0x10003\n"
    "s_lshl1_add_u32 s69, s55, s57\n"
    "s_cmp_lt_u32 s56, 10\n"
    "s_cselect_b32 s55, s55, s69\n"
    "s_cselect_b32 s57, s57, 1\n"
    "s_lshl_b32 s69, s55, 5\n"
    "s_sub_u32 s56, s55, 12\n"
    "s_mul_i32 s56, s56, 96\n"
    "s_cmp_lt_u32 s55, 12\n"
    "s_cselect_b32 s69, s69, s56\n"
    "s_cselect_b32 s56, s57, 0\n"
    "s_cselect_b32 s62, 0, 21\n"
    "s_mul_i32 s98, s56, 110592\n"
    "s_add_u32 s70, s70, s56\n"
    "s_mul_i32 s56, s56, 147456\n"
    "s_sub_u32 s52, 73728, s56\n"
    "v_mul_u32_u24 v4, 1366, v3\n"
    "v_lshrrev_b32 v2, 11, v2\n"
    "v_lshrrev_b32 v4, 11, v4\n"
    "s_mul_i32 s47, s70, 73728\n"
    "s_mul_i32 s55, s69, 192\n"
    "s_add_u32 s48, s47, s55\n"
    "s_sub_u32 s48, s48, 73728\n"
    "s_add_u32 s59, s48, s52\n"
    "s_add_u32 s47, s47, 192\n"
    "v_and_b32 v2, 32, v2\n"
    "v_and_b32 v4, 32, v4\n"
    "v_lshlrev_b32 v1, 4, v1\n"
    "v_lshlrev_b32 v3, 4, v3\n"
    "s_lshl_b32 s51, s58, 4\n"
    "s_add_u32 s51, s51, 20224\n"
    "s_mov_b32 s49, s51\n"
    "v_xor_b32 v176, v1, v2\n"
    "v_xor_b32 v175, v3, v4\n"
    "s_or_b32 s55, s69, %2\n"
    "s_cselect_b32 s72, 0, 0xfff\n"
    "s_mov_b32 s73, 0\n"
    "v_mov_b32 v2, 0x40000000\n"
    "v_cndmask_b32 v174, v176, v2, s[72:73]\n"
    "s_mov_b32 s27, 0x20000\n"
    "s_mov_b32 s26, s47\n"
    "s_mov_b32 m0, s49\n"
    "v_lshlrev_b32 v177, 4, v0\n"
    "v_xor_b32 v179, 32, v177\n"
    "s_mov_b32 s72, 0\n"
    "s_mov_b32 s73, -1\n"
    "v_cndmask_b32 v178, v177, v179, s[72:73]\n"
    "s_mul_i32 s66, %2, 3072\n"
    "s_add_u32 s65, s66, 65536\n"
    "s_add_u32 s96, s66, s98\n"
    "s_sub_u32 s98, s66, s98\n"
    "s_mov_b32 s30, 165888\n"
    "s_mov_b32 s31, 0x20000\n"
    "s_mov_b32 s42, 192\n"
    "s_mov_b32 s43, 0x20000\n"
    "s_and_b32 s67, %2, 1\n"
    "s_mul_i32 s67, s67, 96\n"
    "v_lshrrev_b32 v7, 4, v0\n"
    "v_lshlrev_b32 v5, 3, v7\n"
    "v_add_u32 v184, s67, v5\n"
    "s_lshr_b32 s68, %2, 1\n"
    "s_lshl_b32 s68, s68, 4\n"
    "v_and_b32 v6, 15, v0\n"
    "v_add_u32 v6, s68, v6\n"
    "v_lshlrev_b32 v4, 4, v7\n"
    "v_add_u32 v1, 0, v6\n"
    "v_lshlrev_b32 v2, 3, v1\n"
    "v_and_b32 v2, 32, v2\n"
    "v_xor_b32 v2, v2, v4\n"
    "v_mul_u32_u24 v1, 192, v1\n"
    "v_add_u32 v1, 20224, v1\n"
    "v_add_u32 v166, v1, v2\n"
    "v_add_u32 v1, 1, v6\n"
    "v_lshlrev_b32 v2, 3, v1\n"
    "v_and_b32 v2, 32, v2\n"
    "v_xor_b32 v2, v2, v4\n"
    "v_mul_u32_u24 v1, 192, v1\n"
    "v_add_u32 v1, 20224, v1\n"
    "v_add_u32 v167, v1, v2\n"
    "v_add_u32 v1, 2, v6\n"
    "v_lshlrev_b32 v2, 3, v1\n"
    "v_and_b32 v2, 32, v2\n"
    "v_xor_b32 v2, v2, v4\n"
    "v_mul_u32_u24 v1, 192, v1\n"
    "v_add_u32 v1, 20224, v1\n"
    "v_add_u32 v168, v1, v2\n"
    "v_bfe_u32 v1, v0, 2, 2\n"
    "v_add_u32 v1, v5, v1\n"
    "v_mul_u32_u24 v1, 192, v1\n"
    "v_and_b32 v2, 3, v0\n"
    "v_lshl_add_u32 v1, v2, 3, v1\n"
    "v_add_u32 v1, 65536, v1\n"
    "v_and_b32 v2, 1, v7\n"
    "v_lshlrev_b32 v2, 5, v2\n"
    "v_xor_b32 v3, s67, v2\n"
    "v_add_u32 v180, v1, v3\n"
    "s_add_u32 s61, s67, 32\n"
    "v_xor_b32 v3, s61, v2\n"
    "v_add_u32 v181, v1, v3\n"
    "v_add_u32 v182, 49152, v180\n"
    "v_add_u32 v183, 49152, v181\n"
    "s_waitcnt lgkmcnt(0)\n"
    "s_sub_u32 s24, s16, 192\n"
    "s_subb_u32 s25, s17, 0\n"
    "s_and_b32 s25, s25, 0xffff\n"
    "buffer_load_dwordx4 v174, s[24:27], s48 offen lds\n"
    "s_mov_b32 s28, s18\n"
    "s_and_b32 s29, s19, 0xffff\n"
    "s_mov_b32 s40, s20\n"
    "s_and_b32 s41, s21, 0xffff\n"
    "s_add_u32 m0, s49, s71\n"
    "s_nop 0\n"
    "buffer_load_dwordx4 v175, s[24:27], s48 offen lds\n"
    "s_add_u32 s47, s47, s52\n"
    "s_add_u32 s48, s48, s52\n"
    "s_add_u32 s49, s49, 6528\n"
    "s_mov_b32 s26, s47\n"
    "s_mov_b32 m0, s49\n"
    "s_nop 0\n"
    "buffer_load_dwordx4 v174, s[24:27], s48 offen lds\n"
    "s_add_u32 m0, s49, s71\n"
    "s_nop 0\n"
    "buffer_load_dwordx4 v175, s[24:27], s48 offen lds\n"
    "s_add_u32 s47, s47, s52\n"
    "s_add_u32 s48, s48, s52\n"
    "s_add_u32 s49, s49, 6528\n"
    "s_add_u32 m0, s65, 0\n"
    "s_add_u32 s57, s96, 0\n"
    "buffer_load_dwordx4 v177, s[28:31], s57 offen offset:0 lds\n"
    "buffer_load_dwordx4 v178, s[28:31], s57 offen offset:1024 lds\n"
    "buffer_load_dwordx4 v179, s[28:31], s57 offen offset:2048 lds\n"
    "s_lshr_b32 s55, %1, 3\n"
    "s_lshl_b32 s55, s55, 2\n"
    "s_add_u32 s55, s55, %2\n"
    "s_mul_i32 s55, s55, 1408\n"
    "s_add_u32 s55, s55, 0\n"
    "v_min_u32 v156, 160, v177\n"
    "v_lshlrev_b32 v156, 3, v156\n"
    "buffer_load_dword v158, v156, s[28:31], s55 offen\n"
    "s_mov_b32 s26, s47\n"
    "s_mov_b32 m0, s49\n"
    "s_nop 0\n"
    "buffer_load_dwordx4 v174, s[24:27], s48 offen lds\n"
    "s_add_u32 m0, s49, s71\n"
    "s_nop 0\n"
    "buffer_load_dwordx4 v175, s[24:27], s48 offen lds\n"
    "s_add_u32 s47, s47, s52\n"
    "s_add_u32 s48, s48, s52\n"
    "s_add_u32 s49, s49, 6528\n"
    "v_mul_u32_u24 v1, 200, v6\n"
    "v_add_u32 v169, v1, v184\n"
    "s_mul_i32 s61, %2, 1536\n"
    "s_mul_i32 s68, %2, 1600\n"
    "v_lshlrev_b32 v1, 2, v0\n"
    "v_add_u32 v173, s61, v1\n"
    "v_add_u32 v2, 0, v1\n"
    "v_mul_u32_u24 v3, 2731, v2\n"
    "v_lshrrev_b32 v3, 19, v3\n"
    "v_lshl_add_u32 v2, v3, 3, v2\n"
    "v_add_u32 v170, s68, v2\n"
    "v_add_u32 v2, 256, v1\n"
    "v_mul_u32_u24 v3, 2731, v2\n"
    "v_lshrrev_b32 v3, 19, v3\n"
    "v_lshl_add_u32 v2, v3, 3, v2\n"
    "v_add_u32 v171, s68, v2\n"
    "v_add_u32 v2, 512, v1\n"
    "v_mul_u32_u24 v3, 2731, v2\n"
    "v_lshrrev_b32 v3, 19, v3\n"
    "v_lshl_add_u32 v2, v3, 3, v2\n"
    "v_add_u32 v172, s68, v2\n"
    "s_mov_b32 s36, s22\n"
    "s_and_b32 s37, s23, 0xffff\n"
    "s_mov_b32 s38, 28311552\n"
    "s_mov_b32 s39, 0x20000\n"
    "s_mov_b32 s60, s52\n"
    "s_mov_b32 s61, 0\n"
    "s_mov_b32 s53, 0x40000000\n"
    "s_cmp_eq_u32 s62, 0\n"
    "s_cselect_b32 s44, 10, 3\n"
    "s_cselect_b32 s53, s53, 4294524928\n"
    "s_add_u32 s54, s53, 6144\n"
    "s_mul_i32 s99, s52, -5\n"
    "s_add_u32 s99, s99, 6144\n"
    "s_add_u32 s50, s51, 137088\n"
    "s_cmp_lt_u32 %2, 2\n"
    "s_cselect_b32 s97, s96, s66\n"
    "s_cmp_eq_u32 s62, 0\n"
    "s_cselect_b32 s72, -1, 0\n"
    "s_mov_b32 s73, s72\n"
    "v_cndmask_b32 v176, v176, v174, s[72:73]\n"
    "v_lshlrev_b32 v1, 2, v0\n"
    "s_lshl_b32 s68, %2, 8\n"
    "s_add_u32 s68, s68, 19200\n"
    "v_add_u32 v186, s68, v1\n"
    "v_and_b32 v2, 3, v0\n"
    "v_lshl_add_u32 v2, v2, 8, v1\n"
    "v_add_u32 v187, 19200, v2\n"
    "v_mov_b32 v185, 0\n"
    "ds_write_b32 v186, v185\n"
    "s_mov_b64 exec, 0\n"
    "s_cmp_eq_u32 %2, 0\n"
    "s_cbranch_scc1 .Lconv_pad0%=\n"
    "s_cmp_eq_u32 %2, 1\n"
    "s_cbranch_scc1 .Lconv_pad1%=\n"
    "s_cmp_eq_u32 %2, 2\n"
    "s_cbranch_scc1 .Lconv_pad2%=\n"
    "s_branch .Lconv_pad3%=\n"
    ".Lconv_back%=:\n"
    "s_mov_b64 exec, -1\n"
    "s_add_u32 m0, s65, 12288\n"
    "s_add_u32 s57, s96, 12288\n"
    "buffer_load_dwordx4 v177, s[28:31], s57 offen offset:0 lds\n"
    "buffer_load_dwordx4 v178, s[28:31], s57 offen offset:1024 lds\n"
    "buffer_load_dwordx4 v179, s[28:31], s57 offen offset:2048 lds\n"
    "buffer_load_dwordx2 v[160:161], v184, s[40:43], 0 offen offset:0\n"
    "buffer_load_dwordx2 v[162:163], v184, s[40:43], 0 offen offset:32\n"
    "buffer_load_dwordx2 v[164:165], v184, s[40:43], 0 offen offset:64\n"
    "s_add_u32 m0, s65, 24576\n"
    "s_add_u32 s57, s96, 24576\n"
    "buffer_load_dwordx4 v177, s[28:31], s57 offen offset:0 lds\n"
    "buffer_load_dwordx4 v178, s[28:31], s57 offen offset:1024 lds\n"
    "buffer_load_dwordx4 v179, s[28:31], s57 offen offset:2048 lds\n"
    "s_add_u32 m0, s65, 36864\n"
    "s_add_u32 s57, s96, 36864\n"
    "buffer_load_dwordx4 v177, s[28:31], s57 offen offset:0 lds\n"
    "buffer_load_dwordx4 v178, s[28:31], s57 offen offset:1024 lds\n"
    "buffer_load_dwordx4 v179, s[28:31], s57 offen offset:2048 lds\n"
    "s_waitcnt vmcnt(18)\n"
    "s_waitcnt lgkmcnt(0)\n"
    "s_barrier\n"
    "ds_read_b128 v[0:3], v166 offset:0\n"
    "ds_read_b128 v[4:7], v166 offset:64\n"
    "ds_read_b128 v[8:11], v166 offset:128\n"
    "ds_read_b128 v[12:15], v167 offset:0\n"
    "ds_read_b128 v[16:19], v167 offset:64\n"
    "ds_read_b128 v[20:23], v167 offset:128\n"
    "ds_read_b128 v[24:27], v168 offset:0\n"
    "ds_read_b128 v[28:31], v168 offset:64\n"
    "ds_read_b128 v[32:35], v168 offset:128\n"
    "ds_read_b128 v[36:39], v166 offset:6528\n"
    "ds_read_b128 v[40:43], v166 offset:6592\n"
    "ds_read_b128 v[44:47], v166 offset:6656\n"
    "ds_read_b128 v[48:51], v167 offset:6528\n"
    "ds_read_b128 v[52:55], v167 offset:6592\n"
    "ds_read_b128 v[56:59], v167 offset:6656\n"
    "ds_read_b128 v[60:63], v168 offset:6528\n"
    "ds_read_b128 v[64:67], v168 offset:6592\n"
    "ds_read_b128 v[68:71], v168 offset:6656\n"
    "s_waitcnt vmcnt(15)\n"
    "s_barrier\n"
    "ds_read_b64_tr_b16 a[0:1], v180 offset:0\n"
    "ds_read_b64_tr_b16 a[2:3], v180 offset:768\n"
    "ds_read_b64_tr_b16 a[12:13], v180 offset:6144\n"
    "ds_read_b64_tr_b16 a[14:15], v180 offset:6912\n"
    "ds_read_b64_tr_b16 a[4:5], v181 offset:0\n"
    "ds_read_b64_tr_b16 a[6:7], v181 offset:768\n"
    "ds_read_b64_tr_b16 a[16:17], v181 offset:6144\n"
    "ds_read_b64_tr_b16 a[18:19], v181 offset:6912\n"
    "ds_read_b64_tr_b16 a[8:9], v180 offset:64\n"
    "ds_read_b64_tr_b16 a[10:11], v180 offset:832\n"
    "ds_read_b64_tr_b16 a[20:21], v180 offset:6208\n"
    "ds_read_b64_tr_b16 a[22:23], v180 offset:6976\n"
    "s_waitcnt lgkmcnt(10)\n"
    "v_mfma_f32_16x16x32_bf16 v[108:111], a[0:3], v[0:3], 0\n"
    "s_add_u32 m0, s65, 49152\n"
    "s_add_u32 s57, s97, 49152\n"
    "s_waitcnt lgkmcnt(8)\n"
    "v_mfma_f32_16x16x32_bf16 v[108:111], a[12:15], v[4:7], v[108:111]\n"
    "buffer_load_dwordx4 v177, s[28:31], s57 offen offset:0 lds\n"
    "s_waitcnt lgkmcnt(6)\n"
    "v_mfma_f32_16x16x32_bf16 v[112:115], a[4:7], v[0:3], 0\n"
    "s_waitcnt lgkmcnt(4)\n"
    "v_mfma_f32_16x16x32_bf16 v[112:115], a[16:19], v[4:7], v[112:115]\n"
    "s_waitcnt lgkmcnt(2)\n"
    "v_mfma_f32_16x16x32_bf16 v[116:119], a[8:11], v[0:3], 0\n"
    "buffer_load_dwordx4 v178, s[28:31], s57 offen offset:1024 lds\n"
    "s_waitcnt lgkmcnt(0)\n"
    "v_mfma_f32_16x16x32_bf16 v[116:119], a[20:23], v[4:7], v[116:119]\n"
    "v_mfma_f32_16x16x32_bf16 v[120:123], a[0:3], v[36:39], 0\n"
    "v_mfma_f32_16x16x32_bf16 v[120:123], a[12:15], v[40:43], v[120:123]\n"
    "s_waitcnt vmcnt(11)\n"
    "v_mfma_f32_16x16x32_bf16 v[124:127], a[4:7], v[36:39], 0\n"
    "s_barrier\n"
    "v_mfma_f32_16x16x32_bf16 v[124:127], a[16:19], v[40:43], v[124:127]\n"
    "ds_read_b128 v[72:75], v166 offset:13056\n"
    "ds_read_b128 v[76:79], v166 offset:13120\n"
    "v_mfma_f32_16x16x32_bf16 v[128:131], a[8:11], v[36:39], 0\n"
    "ds_read_b64_tr_b16 a[24:25], v180 offset:12288\n"
    "ds_read_b64_tr_b16 a[26:27], v180 offset:13056\n"
    "v_mfma_f32_16x16x32_bf16 v[128:131], a[20:23], v[40:43], v[128:131]\n"
    "ds_read_b64_tr_b16 a[36:37], v180 offset:18432\n"
    "ds_read_b64_tr_b16 a[38:39], v180 offset:19200\n"
    "s_waitcnt lgkmcnt(5)\n"
    "v_mfma_f32_16x16x32_bf16 v[132:135], a[0:3], v[72:75], 0\n"
    "buffer_load_dwordx4 v179, s[28:31], s57 offen offset:2048 lds\n"
    "ds_read_b64_tr_b16 a[28:29], v181 offset:12288\n"
    "s_waitcnt lgkmcnt(5)\n"
    "v_mfma_f32_16x16x32_bf16 v[132:135], a[12:15], v[76:79], v[132:135]\n"
    "s_add_u32 m0, s65, 61440\n"
    "s_add_u32 s57, s66, 61440\n"
    "ds_read_b64_tr_b16 a[30:31], v181 offset:13056\n"
    "ds_read_b64_tr_b16 a[40:41], v181 offset:18432\n"
    "v_mfma_f32_16x16x32_bf16 v[136:139], a[4:7], v[72:75], 0\n"
    "ds_read_b64_tr_b16 a[42:43], v181 offset:19200\n"
    "ds_read_b64_tr_b16 a[32:33], v180 offset:12352\n"
    "v_mfma_f32_16x16x32_bf16 v[136:139], a[16:19], v[76:79], v[136:139]\n"
    "ds_read_b64_tr_b16 a[34:35], v180 offset:13120\n"
    "ds_read_b64_tr_b16 a[44:45], v180 offset:18496\n"
    "v_mfma_f32_16x16x32_bf16 v[140:143], a[8:11], v[72:75], 0\n"
    "ds_read_b64_tr_b16 a[46:47], v180 offset:19264\n"
    "ds_read_b128 v[80:83], v166 offset:13184\n"
    "v_mfma_f32_16x16x32_bf16 v[140:143], a[20:23], v[76:79], v[140:143]\n"
    "buffer_load_dwordx4 v177, s[28:31], s57 offen offset:0 lds\n"
    "s_waitcnt lgkmcnt(11)\n"
    "v_mfma_f32_16x16x32_bf16 v[108:111], a[24:27], v[8:11], v[108:111]\n"
    "ds_read_b128 v[84:87], v167 offset:13056\n"
    "s_waitcnt lgkmcnt(10)\n"
    "v_mfma_f32_16x16x32_bf16 v[108:111], a[36:39], v[12:15], v[108:111]\n"
    "buffer_load_dwordx4 v178, s[28:31], s57 offen offset:1024 lds\n"
    "ds_read_b128 v[88:91], v167 offset:13120\n"
    "s_waitcnt lgkmcnt(9)\n"
    "v_mfma_f32_16x16x32_bf16 v[112:115], a[28:31], v[8:11], v[112:115]\n"
    "s_waitcnt vmcnt(5)\n"
    "s_waitcnt lgkmcnt(7)\n"
    "v_mfma_f32_16x16x32_bf16 v[112:115], a[40:43], v[12:15], v[112:115]\n"
    "s_barrier\n"
    "s_waitcnt lgkmcnt(5)\n"
    "v_mfma_f32_16x16x32_bf16 v[116:119], a[32:35], v[8:11], v[116:119]\n"
    "buffer_load_dwordx4 v179, s[28:31], s57 offen offset:2048 lds\n"
    "ds_read_b64_tr_b16 a[48:49], v180 offset:24576\n"
    "s_waitcnt lgkmcnt(4)\n"
    "v_mfma_f32_16x16x32_bf16 v[116:119], a[44:47], v[12:15], v[116:119]\n"
    "ds_read_b64_tr_b16 a[50:51], v180 offset:25344\n"
    "ds_read_b64_tr_b16 a[60:61], v180 offset:30720\n"
    "v_mfma_f32_16x16x32_bf16 v[120:123], a[24:27], v[44:47], v[120:123]\n"
    "ds_read_b64_tr_b16 a[62:63], v180 offset:31488\n"
    "ds_read_b64_tr_b16 a[52:53], v181 offset:24576\n"
    "v_mfma_f32_16x16x32_bf16 v[120:123], a[36:39], v[48:51], v[120:123]\n"
    "s_add_u32 m0, s65, 73728\n"
    "s_add_u32 s57, s66, 73728\n"
    "ds_read_b64_tr_b16 a[54:55], v181 offset:25344\n"
    "v_mfma_f32_16x16x32_bf16 v[124:127], a[28:31], v[44:47], v[124:127]\n"
    "buffer_load_dwordx4 v177, s[28:31], s57 offen offset:0 lds\n"
    "v_mfma_f32_16x16x32_bf16 v[124:127], a[40:43], v[48:51], v[124:127]\n"
    "ds_read_b64_tr_b16 a[64:65], v181 offset:30720\n"
    "v_mfma_f32_16x16x32_bf16 v[128:131], a[32:35], v[44:47], v[128:131]\n"
    "ds_read_b64_tr_b16 a[66:67], v181 offset:31488\n"
    "v_mfma_f32_16x16x32_bf16 v[128:131], a[44:47], v[48:51], v[128:131]\n"
    "ds_read_b64_tr_b16 a[56:57], v180 offset:24640\n"
    "s_waitcnt lgkmcnt(8)\n"
    "v_mfma_f32_16x16x32_bf16 v[132:135], a[24:27], v[80:83], v[132:135]\n"
    "ds_read_b64_tr_b16 a[58:59], v180 offset:25408\n"
    "v_mfma_f32_16x16x32_bf16 v[132:135], a[36:39], v[84:87], v[132:135]\n"
    "buffer_load_dwordx4 v178, s[28:31], s57 offen offset:1024 lds\n"
    "v_mfma_f32_16x16x32_bf16 v[136:139], a[28:31], v[80:83], v[136:139]\n"
    "ds_read_b64_tr_b16 a[68:69], v180 offset:30784\n"
    "v_mfma_f32_16x16x32_bf16 v[136:139], a[40:43], v[84:87], v[136:139]\n"
    "ds_read_b64_tr_b16 a[70:71], v180 offset:31552\n"
    "v_mfma_f32_16x16x32_bf16 v[140:143], a[32:35], v[80:83], v[140:143]\n"
    "buffer_load_dwordx4 v179, s[28:31], s57 offen offset:2048 lds\n"
    "v_mfma_f32_16x16x32_bf16 v[140:143], a[44:47], v[84:87], v[140:143]\n"
    "ds_read_b128 v[92:95], v167 offset:13184\n"
    "s_waitcnt lgkmcnt(5)\n"
    "v_mfma_f32_16x16x32_bf16 v[108:111], a[48:51], v[16:19], v[108:111]\n"
    "ds_read_b64_tr_b16 a[72:73], v180 offset:36864\n"
    "v_mfma_f32_16x16x32_bf16 v[108:111], a[60:63], v[20:23], v[108:111]\n"
    "s_add_u32 m0, s65, 86016\n"
    "s_add_u32 s57, s66, 86016\n"
    "ds_read_b64_tr_b16 a[74:75], v180 offset:37632\n"
    "v_mfma_f32_16x16x32_bf16 v[112:115], a[52:55], v[16:19], v[112:115]\n"
    "buffer_load_dwordx4 v177, s[28:31], s57 offen offset:0 lds\n"
    "v_mfma_f32_16x16x32_bf16 v[112:115], a[64:67], v[20:23], v[112:115]\n"
    "ds_read_b64_tr_b16 a[84:85], v180 offset:43008\n"
    "s_waitcnt lgkmcnt(5)\n"
    "v_mfma_f32_16x16x32_bf16 v[116:119], a[56:59], v[16:19], v[116:119]\n"
    "ds_read_b64_tr_b16 a[86:87], v180 offset:43776\n"
    "s_waitcnt lgkmcnt(5)\n"
    "v_mfma_f32_16x16x32_bf16 v[116:119], a[68:71], v[20:23], v[116:119]\n"
    "ds_read_b64_tr_b16 a[76:77], v181 offset:36864\n"
    "v_mfma_f32_16x16x32_bf16 v[120:123], a[48:51], v[52:55], v[120:123]\n"
    "buffer_load_dwordx4 v178, s[28:31], s57 offen offset:1024 lds\n"
    "v_mfma_f32_16x16x32_bf16 v[120:123], a[60:63], v[56:59], v[120:123]\n"
    "ds_read_b64_tr_b16 a[78:79], v181 offset:37632\n"
    "v_mfma_f32_16x16x32_bf16 v[124:127], a[52:55], v[52:55], v[124:127]\n"
    "ds_read_b64_tr_b16 a[88:89], v181 offset:43008\n"
    "v_mfma_f32_16x16x32_bf16 v[124:127], a[64:67], v[56:59], v[124:127]\n"
    "ds_read_b64_tr_b16 a[90:91], v181 offset:43776\n"
    "v_mfma_f32_16x16x32_bf16 v[128:131], a[56:59], v[52:55], v[128:131]\n"
    "buffer_load_dwordx4 v179, s[28:31], s57 offen offset:2048 lds\n"
    "v_mfma_f32_16x16x32_bf16 v[128:131], a[68:71], v[56:59], v[128:131]\n"
    "ds_read_b64_tr_b16 a[80:81], v180 offset:36928\n"
    "v_mfma_f32_16x16x32_bf16 v[132:135], a[48:51], v[88:91], v[132:135]\n"
    "s_waitcnt vmcnt(6)\n"
    "s_waitcnt lgkmcnt(4)\n"
    "v_mfma_f32_16x16x32_bf16 v[132:135], a[60:63], v[92:95], v[132:135]\n"
    "s_barrier\n"
    "v_mfma_f32_16x16x32_bf16 v[136:139], a[52:55], v[88:91], v[136:139]\n"
    "s_add_u32 m0, s65, 0\n"
    "s_add_u32 s57, s66, 98304\n"
    "ds_read_b64_tr_b16 a[82:83], v180 offset:37696\n"
    "ds_read_b64_tr_b16 a[92:93], v180 offset:43072\n"
    "v_mfma_f32_16x16x32_bf16 v[136:139], a[64:67], v[92:95], v[136:139]\n"
    "ds_read_b64_tr_b16 a[94:95], v180 offset:43840\n"
    "v_lshlrev_b32 v144, 16, v160\n"
    "v_mfma_f32_16x16x32_bf16 v[140:143], a[56:59], v[88:91], v[140:143]\n"
    "buffer_load_dwordx4 v177, s[28:31], s57 offen offset:0 lds\n"
    "v_and_b32 v145, 0xffff0000, v160\n"
    "v_mfma_f32_16x16x32_bf16 v[140:143], a[68:71], v[92:95], v[140:143]\n"
    "ds_read_b128 v[96:99], v168 offset:13056\n"
    "v_lshlrev_b32 v146, 16, v161\n"
    "v_mfma_f32_16x16x32_bf16 v[108:111], a[72:75], v[24:27], v[108:111]\n"
    "buffer_load_dwordx4 v178, s[28:31], s57 offen offset:1024 lds\n"
    "v_and_b32 v147, 0xffff0000, v161\n"
    "v_mfma_f32_16x16x32_bf16 v[108:111], a[84:87], v[28:31], v[108:111]\n"
    "ds_read_b128 v[100:103], v168 offset:13120\n"
    "v_lshlrev_b32 v148, 16, v162\n"
    "s_waitcnt lgkmcnt(5)\n"
    "v_mfma_f32_16x16x32_bf16 v[112:115], a[76:79], v[24:27], v[112:115]\n"
    "ds_read_b64_tr_b16 a[96:97], v182 offset:0\n"
    "v_and_b32 v149, 0xffff0000, v162\n"
    "v_mfma_f32_16x16x32_bf16 v[112:115], a[88:91], v[28:31], v[112:115]\n"
    "ds_read_b64_tr_b16 a[98:99], v182 offset:768\n"
    "v_lshlrev_b32 v150, 16, v163\n"
    "s_waitcnt lgkmcnt(5)\n"
    "v_mfma_f32_16x16x32_bf16 v[116:119], a[80:83], v[24:27], v[116:119]\n"
    "buffer_load_dwordx4 v179, s[28:31], s57 offen offset:2048 lds\n"
    "v_and_b32 v151, 0xffff0000, v163\n"
    "s_waitcnt lgkmcnt(4)\n"
    "v_mfma_f32_16x16x32_bf16 v[116:119], a[92:95], v[28:31], v[116:119]\n"
    "ds_read_b64_tr_b16 a[100:101], v183 offset:0\n"
    "v_lshlrev_b32 v152, 16, v164\n"
    "v_mfma_f32_16x16x32_bf16 v[120:123], a[72:75], v[60:63], v[120:123]\n"
    "ds_read_b64_tr_b16 a[102:103], v183 offset:768\n"
    "v_and_b32 v153, 0xffff0000, v164\n"
    "v_mfma_f32_16x16x32_bf16 v[120:123], a[84:87], v[64:67], v[120:123]\n"
    "ds_read_b64_tr_b16 a[104:105], v182 offset:64\n"
    "v_lshlrev_b32 v154, 16, v165\n"
    "v_mfma_f32_16x16x32_bf16 v[124:127], a[76:79], v[60:63], v[124:127]\n"
    "ds_read_b64_tr_b16 a[106:107], v182 offset:832\n"
    "v_and_b32 v155, 0xffff0000, v165\n"
    "v_mfma_f32_16x16x32_bf16 v[124:127], a[88:91], v[64:67], v[124:127]\n"
    "s_mov_b32 s26, s47\n"
    "ds_read_b128 v[104:107], v168 offset:13184\n"
    "ds_read_b64_tr_b16 a[108:109], v182 offset:6144\n"
    "v_mfma_f32_16x16x32_bf16 v[128:131], a[80:83], v[60:63], v[128:131]\n"
    "s_mov_b32 m0, s49\n"
    "ds_read_b64_tr_b16 a[110:111], v182 offset:6912\n"
    "ds_read_b64_tr_b16 a[120:121], v182 offset:12288\n"
    "v_mfma_f32_16x16x32_bf16 v[128:131], a[92:95], v[64:67], v[128:131]\n"
    "buffer_load_dwordx4 v174, s[24:27], s48 offen lds\n"
    "ds_read_b64_tr_b16 a[122:123], v182 offset:13056\n"
    "s_waitcnt lgkmcnt(9)\n"
    "v_mfma_f32_16x16x32_bf16 v[132:135], a[72:75], v[96:99], v[132:135]\n"
    "ds_read_b64_tr_b16 a[132:133], v182 offset:18432\n"
    "ds_read_b64_tr_b16 a[134:135], v182 offset:19200\n"
    "v_mfma_f32_16x16x32_bf16 v[132:135], a[84:87], v[100:103], v[132:135]\n"
    "s_add_u32 m0, s49, s71\n"
    "ds_read_b64_tr_b16 a[112:113], v183 offset:6144\n"
    "v_mfma_f32_16x16x32_bf16 v[136:139], a[76:79], v[96:99], v[136:139]\n"
    "buffer_load_dwordx4 v175, s[24:27], s48 offen lds\n"
    "v_mfma_f32_16x16x32_bf16 v[136:139], a[88:91], v[100:103], v[136:139]\n"
    "ds_read_b64_tr_b16 a[114:115], v183 offset:6912\n"
    "v_mfma_f32_16x16x32_bf16 v[140:143], a[80:83], v[96:99], v[140:143]\n"
    "ds_read_b64_tr_b16 a[124:125], v183 offset:12288\n"
    "ds_read_b64_tr_b16 a[126:127], v183 offset:13056\n"
    "v_mfma_f32_16x16x32_bf16 v[140:143], a[92:95], v[100:103], v[140:143]\n"
    "s_add_u32 s47, s47, s52\n"
    "s_add_u32 s48, s48, s52\n"
    "s_add_u32 s49, s49, 6528\n"
    "ds_read_b64_tr_b16 a[136:137], v183 offset:18432\n"
    "v_mfma_f32_16x16x32_bf16 v[108:111], a[96:99], v[32:35], v[108:111]\n"
    "s_add_u32 m0, s65, 12288\n"
    "s_add_u32 s57, s98, 110592\n"
    "ds_read_b64_tr_b16 a[138:139], v183 offset:19200\n"
    "s_waitcnt lgkmcnt(8)\n"
    "v_mfma_f32_16x16x32_bf16 v[112:115], a[100:103], v[32:35], v[112:115]\n"
    "buffer_load_dwordx4 v177, s[28:31], s57 offen offset:0 lds\n"
    "v_mfma_f32_16x16x32_bf16 v[116:119], a[104:107], v[32:35], v[116:119]\n"
    "ds_read_b64_tr_b16 a[116:117], v182 offset:6208\n"
    "ds_read_b64_tr_b16 a[118:119], v182 offset:6976\n"
    "v_mfma_f32_16x16x32_bf16 v[120:123], a[96:99], v[68:71], v[120:123]\n"
    "buffer_load_dwordx4 v178, s[28:31], s57 offen offset:1024 lds\n"
    "v_mfma_f32_16x16x32_bf16 v[124:127], a[100:103], v[68:71], v[124:127]\n"
    "ds_read_b64_tr_b16 a[128:129], v182 offset:12352\n"
    "v_mfma_f32_16x16x32_bf16 v[128:131], a[104:107], v[68:71], v[128:131]\n"
    "ds_read_b64_tr_b16 a[130:131], v182 offset:13120\n"
    "v_mfma_f32_16x16x32_bf16 v[132:135], a[96:99], v[104:107], v[132:135]\n"
    "s_waitcnt vmcnt(4)\n"
    "v_mfma_f32_16x16x32_bf16 v[136:139], a[100:103], v[104:107], v[136:139]\n"
    "s_barrier\n"
    "v_mfma_f32_16x16x32_bf16 v[140:143], a[104:107], v[104:107], v[140:143]\n"
    "ds_read_b64_tr_b16 a[140:141], v182 offset:18496\n"
    "ds_read_b64_tr_b16 a[142:143], v182 offset:19264\n"
    "v_mfma_f32_16x16x32_bf16 v[108:111], a[108:111], v[36:39], v[108:111]\n"
    "buffer_load_dwordx4 v179, s[28:31], s57 offen offset:2048 lds\n"
    "ds_read_b64_tr_b16 a[144:145], v182 offset:24576\n"
    "v_mfma_f32_16x16x32_bf16 v[108:111], a[120:123], v[40:43], v[108:111]\n"
    "ds_read_b64_tr_b16 a[146:147], v182 offset:25344\n"
    "ds_read_b64_tr_b16 a[156:157], v182 offset:30720\n"
    "s_waitcnt lgkmcnt(7)\n"
    "v_mfma_f32_16x16x32_bf16 v[108:111], a[132:135], v[44:47], v[108:111]\n"
    "ds_read_b64_tr_b16 a[158:159], v182 offset:31488\n"
    "ds_read_b64_tr_b16 a[168:169], v182 offset:36864\n"
    "v_mfma_f32_16x16x32_bf16 v[112:115], a[112:115], v[36:39], v[112:115]\n"
    "s_mov_b32 s26, s47\n"
    "ds_read_b64_tr_b16 a[170:171], v182 offset:37632\n"
    "v_mfma_f32_16x16x32_bf16 v[112:115], a[124:127], v[40:43], v[112:115]\n"
    "s_mov_b32 m0, s49\n"
    "ds_read_b64_tr_b16 a[148:149], v183 offset:24576\n"
    "v_mfma_f32_16x16x32_bf16 v[112:115], a[136:139], v[44:47], v[112:115]\n"
    "buffer_load_dwordx4 v174, s[24:27], s48 offen lds\n"
    "ds_read_b64_tr_b16 a[150:151], v183 offset:25344\n"
    "v_mfma_f32_16x16x32_bf16 v[116:119], a[116:119], v[36:39], v[116:119]\n"
    "ds_read_b64_tr_b16 a[160:161], v183 offset:30720\n"
    "s_waitcnt lgkmcnt(9)\n"
    "v_mfma_f32_16x16x32_bf16 v[116:119], a[128:131], v[40:43], v[116:119]\n"
    "s_add_u32 m0, s49, s71\n"
    "ds_read_b64_tr_b16 a[162:163], v183 offset:31488\n"
    "v_mfma_f32_16x16x32_bf16 v[116:119], a[140:143], v[44:47], v[116:119]\n"
    "buffer_load_dwordx4 v175, s[24:27], s48 offen lds\n"
    "v_mfma_f32_16x16x32_bf16 v[120:123], a[108:111], v[72:75], v[120:123]\n"
    "ds_read_b64_tr_b16 a[172:173], v183 offset:36864\n"
    "ds_read_b64_tr_b16 a[174:175], v183 offset:37632\n"
    "v_mfma_f32_16x16x32_bf16 v[120:123], a[120:123], v[76:79], v[120:123]\n"
    "ds_read_b64_tr_b16 a[152:153], v182 offset:24640\n"
    "v_add_f32 v132, v132, v144\n"
    "v_mfma_f32_16x16x32_bf16 v[120:123], a[132:135], v[80:83], v[120:123]\n"
    "s_add_u32 s47, s47, s52\n"
    "s_add_u32 s48, s48, s52\n"
    "s_add_u32 s49, s49, 6528\n"
    "ds_read_b64_tr_b16 a[154:155], v182 offset:25408\n"
    "v_mfma_f32_16x16x32_bf16 v[124:127], a[112:115], v[72:75], v[124:127]\n"
    "s_add_u32 m0, s65, 24576\n"
    "s_add_u32 s57, s98, 122880\n"
    "ds_read_b64_tr_b16 a[164:165], v182 offset:30784\n"
    "v_mfma_f32_16x16x32_bf16 v[124:127], a[124:127], v[76:79], v[124:127]\n"
    "buffer_load_dwordx4 v177, s[28:31], s57 offen offset:0 lds\n"
    "v_add_f32 v133, v133, v145\n"
    "v_mfma_f32_16x16x32_bf16 v[124:127], a[136:139], v[80:83], v[124:127]\n"
    "ds_read_b64_tr_b16 a[166:167], v182 offset:31552\n"
    "v_add_f32 v134, v134, v146\n"
    "v_mfma_f32_16x16x32_bf16 v[128:131], a[116:119], v[72:75], v[128:131]\n"
    "ds_read_b64_tr_b16 a[176:177], v182 offset:36928\n"
    "v_add_f32 v135, v135, v147\n"
    "v_mfma_f32_16x16x32_bf16 v[128:131], a[128:131], v[76:79], v[128:131]\n"
    "ds_read_b64_tr_b16 a[178:179], v182 offset:37696\n"
    "v_add_f32 v136, v136, v148\n"
    "v_mfma_f32_16x16x32_bf16 v[128:131], a[140:143], v[80:83], v[128:131]\n"
    "buffer_load_dwordx4 v178, s[28:31], s57 offen offset:1024 lds\n"
    "v_add_f32 v137, v137, v149\n"
    "s_waitcnt lgkmcnt(5)\n"
    "v_mfma_f32_16x16x32_bf16 v[108:111], a[144:147], v[48:51], v[108:111]\n"
    "ds_read_b64_tr_b16 a[180:181], v182 offset:43008\n"
    "v_add_f32 v138, v138, v150\n"
    "v_mfma_f32_16x16x32_bf16 v[108:111], a[156:159], v[52:55], v[108:111]\n"
    "buffer_load_dwordx4 v179, s[28:31], s57 offen offset:2048 lds\n"
    "v_add_f32 v139, v139, v151\n"
    "v_mfma_f32_16x16x32_bf16 v[108:111], a[168:171], v[56:59], v[108:111]\n"
    "ds_read_b64_tr_b16 a[182:183], v182 offset:43776\n"
    "v_add_f32 v140, v140, v152\n"
    "v_mfma_f32_16x16x32_bf16 v[112:115], a[148:151], v[48:51], v[112:115]\n"
    "ds_read_b64_tr_b16 a[192:193], v180 offset:0\n"
    "v_add_f32 v141, v141, v153\n"
    "v_mfma_f32_16x16x32_bf16 v[112:115], a[160:163], v[52:55], v[112:115]\n"
    "ds_read_b64_tr_b16 a[194:195], v180 offset:768\n"
    "v_add_f32 v142, v142, v154\n"
    "v_mfma_f32_16x16x32_bf16 v[112:115], a[172:175], v[56:59], v[112:115]\n"
    "ds_read_b64_tr_b16 a[204:205], v180 offset:6144\n"
    "v_add_f32 v143, v143, v155\n"
    "s_waitcnt lgkmcnt(5)\n"
    "v_mfma_f32_16x16x32_bf16 v[116:119], a[152:155], v[48:51], v[116:119]\n"
    "ds_read_b64_tr_b16 a[206:207], v180 offset:6912\n"
    "v_mfma_f32_16x16x32_bf16 v[116:119], a[164:167], v[52:55], v[116:119]\n"
    "ds_read_b64_tr_b16 a[184:185], v183 offset:43008\n"
    "v_mfma_f32_16x16x32_bf16 v[116:119], a[176:179], v[56:59], v[116:119]\n"
    "ds_read_b64_tr_b16 a[186:187], v183 offset:43776\n"
    "v_mfma_f32_16x16x32_bf16 v[120:123], a[144:147], v[84:87], v[120:123]\n"
    "ds_read_b64_tr_b16 a[196:197], v181 offset:0\n"
    "v_mfma_f32_16x16x32_bf16 v[120:123], a[156:159], v[88:91], v[120:123]\n"
    "ds_read_b64_tr_b16 a[198:199], v181 offset:768\n"
    "v_mfma_f32_16x16x32_bf16 v[120:123], a[168:171], v[92:95], v[120:123]\n"
    "ds_read_b64_tr_b16 a[208:209], v181 offset:6144\n"
    "v_mfma_f32_16x16x32_bf16 v[124:127], a[148:151], v[84:87], v[124:127]\n"
    "ds_read_b64_tr_b16 a[210:211], v181 offset:6912\n"
    "v_mfma_f32_16x16x32_bf16 v[124:127], a[160:163], v[88:91], v[124:127]\n"
    "ds_read_b64_tr_b16 a[188:189], v182 offset:43072\n"
    "v_mfma_f32_16x16x32_bf16 v[124:127], a[172:175], v[92:95], v[124:127]\n"
    "ds_read_b64_tr_b16 a[190:191], v182 offset:43840\n"
    "v_mfma_f32_16x16x32_bf16 v[128:131], a[152:155], v[84:87], v[128:131]\n"
    "ds_read_b64_tr_b16 a[200:201], v180 offset:64\n"
    "v_mfma_f32_16x16x32_bf16 v[128:131], a[164:167], v[88:91], v[128:131]\n"
    "ds_read_b64_tr_b16 a[202:203], v180 offset:832\n"
    "v_mfma_f32_16x16x32_bf16 v[128:131], a[176:179], v[92:95], v[128:131]\n"
    "ds_read_b64_tr_b16 a[212:213], v180 offset:6208\n"
    "s_waitcnt lgkmcnt(7)\n"
    "v_mfma_f32_16x16x32_bf16 v[108:111], a[180:183], v[60:63], v[108:111]\n"
    "ds_read_b64_tr_b16 a[214:215], v180 offset:6976\n"
    ".Lconv_pad0%=:\n"
    "s_cbranch_execz .Lconv_back%=\n"
    "v_mfma_f32_16x16x32_bf16 v[108:111], a[192:195], v[64:67], v[108:111]\n"
    "v_mfma_f32_16x16x32_bf16 v[108:111], a[204:207], v[68:71], v[108:111]\n"
    "v_mfma_f32_16x16x32_bf16 v[112:115], a[184:187], v[60:63], v[112:115]\n"
    "v_mfma_f32_16x16x32_bf16 v[112:115], a[196:199], v[64:67], v[112:115]\n"
    "s_waitcnt lgkmcnt(3)\n"
    "v_mfma_f32_16x16x32_bf16 v[112:115], a[208:211], v[68:71], v[112:115]\n"
    "s_waitcnt vmcnt(5)\n"
    "v_mfma_f32_16x16x32_bf16 v[116:119], a[188:191], v[60:63], v[116:119]\n"
    "s_barrier\n"
    "s_waitcnt lgkmcnt(1)\n"
    "v_mfma_f32_16x16x32_bf16 v[116:119], a[200:203], v[64:67], v[116:119]\n"
    "s_add_u32 m0, s65, 36864\n"
    "s_add_u32 s57, s98, 135168\n"
    "ds_read_b128 v[0:3], v166 offset:19584\n"
    "s_waitcnt lgkmcnt(1)\n"
    "v_mfma_f32_16x16x32_bf16 v[116:119], a[212:215], v[68:71], v[116:119]\n"
    "buffer_load_dwordx4 v177, s[28:31], s57 offen offset:0 lds\n"
    "v_mfma_f32_16x16x32_bf16 v[120:123], a[180:183], v[96:99], v[120:123]\n"
    "ds_read_b128 v[4:7], v166 offset:19648\n"
    "v_mfma_f32_16x16x32_bf16 v[120:123], a[192:195], v[100:103], v[120:123]\n"
    "ds_read_b128 v[8:11], v166 offset:19712\n"
    "v_mfma_f32_16x16x32_bf16 v[120:123], a[204:207], v[104:107], v[120:123]\n"
    "ds_read_b128 v[12:15], v167 offset:19584\n"
    "v_mfma_f32_16x16x32_bf16 v[124:127], a[184:187], v[96:99], v[124:127]\n"
    "buffer_load_dwordx4 v178, s[28:31], s57 offen offset:1024 lds\n"
    "v_mfma_f32_16x16x32_bf16 v[124:127], a[196:199], v[100:103], v[124:127]\n"
    "ds_read_b128 v[16:19], v167 offset:19648\n"
    "v_mfma_f32_16x16x32_bf16 v[124:127], a[208:211], v[104:107], v[124:127]\n"
    "ds_read_b128 v[20:23], v167 offset:19712\n"
    "v_mfma_f32_16x16x32_bf16 v[128:131], a[188:191], v[96:99], v[128:131]\n"
    "ds_read_b128 v[24:27], v168 offset:19584\n"
    "v_mfma_f32_16x16x32_bf16 v[128:131], a[200:203], v[100:103], v[128:131]\n"
    "ds_read_b128 v[28:31], v168 offset:19648\n"
    "v_mfma_f32_16x16x32_bf16 v[128:131], a[212:215], v[104:107], v[128:131]\n"
    "buffer_load_dwordx4 v179, s[28:31], s57 offen offset:2048 lds\n"
    "s_waitcnt lgkmcnt(5)\n"
    "v_mfma_f32_16x16x32_bf16 v[132:135], a[108:111], v[0:3], v[132:135]\n"
    "s_add_u32 m0, s65, 49152\n"
    "s_add_u32 s57, s98, 147456\n"
    "ds_read_b128 v[32:35], v168 offset:19712\n"
    "v_mfma_f32_16x16x32_bf16 v[132:135], a[120:123], v[4:7], v[132:135]\n"
    "buffer_load_dwordx4 v177, s[28:31], s57 offen offset:0 lds\n"
    "v_add_f32 v108, v108, v144\n"
    "v_mfma_f32_16x16x32_bf16 v[132:135], a[132:135], v[8:11], v[132:135]\n"
    "ds_read_b64_tr_b16 a[216:217], v180 offset:12288\n"
    "v_add_f32 v109, v109, v145\n"
    "s_waitcnt lgkmcnt(5)\n"
    "v_mfma_f32_16x16x32_bf16 v[132:135], a[144:147], v[12:15], v[132:135]\n"
    "ds_read_b64_tr_b16 a[218:219], v180 offset:13056\n"
    "v_add_f32 v110, v110, v146\n"
    "v_mfma_f32_16x16x32_bf16 v[132:135], a[156:159], v[16:19], v[132:135]\n"
    "buffer_load_dwordx4 v178, s[28:31], s57 offen offset:1024 lds\n"
    "v_add_f32 v111, v111, v147\n"
    "s_waitcnt lgkmcnt(4)\n"
    "v_mfma_f32_16x16x32_bf16 v[132:135], a[168:171], v[20:23], v[132:135]\n"
    "ds_read_b64_tr_b16 a[228:229], v180 offset:18432\n"
    "v_add_f32 v112, v112, v148\n"
    "v_mfma_f32_16x16x32_bf16 v[132:135], a[180:183], v[24:27], v[132:135]\n"
    "ds_read_b64_tr_b16 a[230:231], v180 offset:19200\n"
    "v_add_f32 v113, v113, v149\n"
    "s_waitcnt lgkmcnt(5)\n"
    "v_mfma_f32_16x16x32_bf16 v[132:135], a[192:195], v[28:31], v[132:135]\n"
    "ds_read_b64_tr_b16 a[220:221], v181 offset:12288\n"
    "v_add_f32 v114, v114, v150\n"
    "s_waitcnt lgkmcnt(5)\n"
    "v_mfma_f32_16x16x32_bf16 v[132:135], a[204:207], v[32:35], v[132:135]\n"
    "buffer_load_dwordx4 v179, s[28:31], s57 offen offset:2048 lds\n"
    "v_add_f32 v115, v115, v151\n"
    "v_mfma_f32_16x16x32_bf16 v[136:139], a[112:115], v[0:3], v[136:139]\n"
    "ds_read_b64_tr_b16 a[222:223], v181 offset:13056\n"
    "v_add_f32 v116, v116, v152\n"
    "v_mfma_f32_16x16x32_bf16 v[136:139], a[124:127], v[4:7], v[136:139]\n"
    "ds_read_b64_tr_b16 a[232:233], v181 offset:18432\n"
    "v_add_f32 v117, v117, v153\n"
    "v_mfma_f32_16x16x32_bf16 v[136:139], a[136:139], v[8:11], v[136:139]\n"
    "ds_read_b64_tr_b16 a[234:235], v181 offset:19200\n"
    "v_add_f32 v118, v118, v154\n"
    "v_mfma_f32_16x16x32_bf16 v[136:139], a[148:151], v[12:15], v[136:139]\n"
    "ds_read_b64_tr_b16 a[224:225], v180 offset:12352\n"
    "v_add_f32 v119, v119, v155\n"
    "v_mfma_f32_16x16x32_bf16 v[136:139], a[160:163], v[16:19], v[136:139]\n"
    "s_add_u32 m0, s65, 61440\n"
    "s_add_u32 s57, s98, 159744\n"
    "ds_read_b64_tr_b16 a[226:227], v180 offset:13120\n"
    "v_mfma_f32_16x16x32_bf16 v[136:139], a[172:175], v[20:23], v[136:139]\n"
    "buffer_load_dwordx4 v177, s[28:31], s57 offen offset:0 lds\n"
    "v_add_f32 v120, v120, v144\n"
    "v_mfma_f32_16x16x32_bf16 v[136:139], a[184:187], v[24:27], v[136:139]\n"
    "s_waitcnt vmcnt(4)\n"
    "v_mfma_f32_16x16x32_bf16 v[136:139], a[196:199], v[28:31], v[136:139]\n"
    "s_barrier\n"
    "v_mfma_f32_16x16x32_bf16 v[136:139], a[208:211], v[32:35], v[136:139]\n"
    "buffer_load_dwordx4 v178, s[28:31], s57 offen offset:1024 lds\n"
    "v_add_f32 v121, v121, v145\n"
    "v_mfma_f32_16x16x32_bf16 v[140:143], a[116:119], v[0:3], v[140:143]\n"
    "ds_read_b64_tr_b16 a[236:237], v180 offset:18496\n"
    "v_add_f32 v122, v122, v146\n"
    "v_mfma_f32_16x16x32_bf16 v[140:143], a[128:131], v[4:7], v[140:143]\n"
    "ds_read_b64_tr_b16 a[238:239], v180 offset:19264\n"
    "v_add_f32 v123, v123, v147\n"
    "v_mfma_f32_16x16x32_bf16 v[140:143], a[140:143], v[8:11], v[140:143]\n"
    "ds_read_b64_tr_b16 a[240:241], v180 offset:24576\n"
    "v_add_f32 v124, v124, v148\n"
    "v_mfma_f32_16x16x32_bf16 v[140:143], a[152:155], v[12:15], v[140:143]\n"
    "buffer_load_dwordx4 v179, s[28:31], s57 offen offset:2048 lds\n"
    "v_add_f32 v125, v125, v149\n"
    "v_mfma_f32_16x16x32_bf16 v[140:143], a[164:167], v[16:19], v[140:143]\n"
    "ds_read_b64_tr_b16 a[242:243], v180 offset:25344\n"
    "v_add_f32 v126, v126, v150\n"
    "v_mfma_f32_16x16x32_bf16 v[140:143], a[176:179], v[20:23], v[140:143]\n"
    "ds_read_b64_tr_b16 a[252:253], v180 offset:30720\n"
    "v_add_f32 v127, v127, v151\n"
    "v_mfma_f32_16x16x32_bf16 v[140:143], a[188:191], v[24:27], v[140:143]\n"
    "ds_read_b64_tr_b16 a[254:255], v180 offset:31488\n"
    "v_add_f32 v128, v128, v152\n"
    "v_mfma_f32_16x16x32_bf16 v[140:143], a[200:203], v[28:31], v[140:143]\n"
    "s_mov_b32 s26, s47\n"
    "v_add_f32 v129, v129, v153\n"
    "v_mfma_f32_16x16x32_bf16 v[140:143], a[212:215], v[32:35], v[140:143]\n"
    "s_mov_b32 m0, s49\n"
    "v_add_f32 v130, v130, v154\n"
    "s_waitcnt lgkmcnt(4)\n"
    "v_mfma_f32_16x16x32_bf16 v[108:111], a[216:219], v[72:75], v[108:111]\n"
    "buffer_load_dwordx4 v174, s[24:27], s48 offen lds\n"
    "v_add_f32 v131, v131, v155\n"
    "v_mfma_f32_16x16x32_bf16 v[108:111], a[228:231], v[76:79], v[108:111]\n"
    "ds_read_b64_tr_b16 v[196:197], v180 offset:36864\n"
    "ds_read_b64_tr_b16 v[198:199], v180 offset:37632\n"
    "v_mfma_f32_16x16x32_bf16 v[112:115], a[220:223], v[72:75], v[112:115]\n"
    "s_add_u32 m0, s49, s71\n"
    "ds_read_b64_tr_b16 v[208:209], v180 offset:43008\n"
    "ds_read_b64_tr_b16 v[210:211], v180 offset:43776\n"
    "v_mfma_f32_16x16x32_bf16 v[112:115], a[232:235], v[76:79], v[112:115]\n"
    "buffer_load_dwordx4 v175, s[24:27], s48 offen lds\n"
    "ds_read_b64_tr_b16 a[244:245], v181 offset:24576\n"
    "v_mfma_f32_16x16x32_bf16 v[116:119], a[224:227], v[72:75], v[116:119]\n"
    "ds_read_b64_tr_b16 a[246:247], v181 offset:25344\n"
    "ds_read_b64_tr_b16 v[188:189], v181 offset:30720\n"
    "v_mfma_f32_16x16x32_bf16 v[116:119], a[236:239], v[76:79], v[116:119]\n"
    "ds_read_b64_tr_b16 v[190:191], v181 offset:31488\n"
    "ds_read_b64_tr_b16 v[200:201], v181 offset:36864\n"
    "v_mfma_f32_16x16x32_bf16 v[120:123], a[216:219], v[0:3], v[120:123]\n"
    "ds_read_b64_tr_b16 v[202:203], v181 offset:37632\n"
    "ds_read_b64_tr_b16 v[212:213], v181 offset:43008\n"
    "v_mfma_f32_16x16x32_bf16 v[120:123], a[228:231], v[4:7], v[120:123]\n"
    "s_add_u32 s47, s47, s52\n"
    "s_add_u32 s48, s48, s52\n"
    "s_add_u32 s49, s49, 6528\n"
    "ds_read_b64_tr_b16 v[214:215], v181 offset:43776\n"
    "v_mfma_f32_16x16x32_bf16 v[124:127], a[220:223], v[0:3], v[124:127]\n"
    "ds_read_b64_tr_b16 a[248:249], v180 offset:24640\n"
    "v_mfma_f32_16x16x32_bf16 v[124:127], a[232:235], v[4:7], v[124:127]\n"
    "ds_read_b64_tr_b16 a[250:251], v180 offset:25408\n"
    "v_mfma_f32_16x16x32_bf16 v[128:131], a[224:227], v[0:3], v[128:131]\n"
    "ds_read_b64_tr_b16 v[192:193], v180 offset:30784\n"
    "v_mfma_f32_16x16x32_bf16 v[128:131], a[236:239], v[4:7], v[128:131]\n"
    "ds_read_b64_tr_b16 v[194:195], v180 offset:31552\n"
    "s_waitcnt lgkmcnt(9)\n"
    "v_mfma_f32_16x16x32_bf16 v[108:111], a[240:243], v[80:83], v[108:111]\n"
    "ds_read_b64_tr_b16 v[204:205], v180 offset:36928\n"
    "v_mfma_f32_16x16x32_bf16 v[108:111], a[252:255], v[84:87], v[108:111]\n"
    "ds_read_b64_tr_b16 v[206:207], v180 offset:37696\n"
    "v_mfma_f32_16x16x32_bf16 v[108:111], v[196:199], v[88:91], v[108:111]\n"
    "ds_read_b64_tr_b16 v[216:217], v180 offset:43072\n"
    "v_mfma_f32_16x16x32_bf16 v[108:111], v[208:211], v[92:95], v[108:111]\n"
    "ds_read_b64_tr_b16 v[218:219], v180 offset:43840\n"
    "v_mfma_f32_16x16x32_bf16 v[112:115], a[244:247], v[80:83], v[112:115]\n"
    "s_waitcnt vmcnt(2)\n"
    "s_waitcnt lgkmcnt(6)\n"
    "v_mfma_f32_16x16x32_bf16 v[112:115], v[188:191], v[84:87], v[112:115]\n"
    "s_barrier\n"
    "v_mfma_f32_16x16x32_bf16 v[112:115], v[200:203], v[88:91], v[112:115]\n"
    "ds_read_b64_tr_b16 v[220:221], v182 offset:0\n"
    "v_mfma_f32_16x16x32_bf16 v[112:115], v[212:215], v[92:95], v[112:115]\n"
    "ds_read_b64_tr_b16 v[222:223], v182 offset:768\n"
    "v_mfma_f32_16x16x32_bf16 v[116:119], a[248:251], v[80:83], v[116:119]\n"
    "ds_read_b64_tr_b16 v[232:233], v182 offset:6144\n"
    "s_waitcnt lgkmcnt(5)\n"
    "v_mfma_f32_16x16x32_bf16 v[116:119], v[192:195], v[84:87], v[116:119]\n"
    "ds_read_b64_tr_b16 v[234:235], v182 offset:6912\n"
    "v_mfma_f32_16x16x32_bf16 v[116:119], v[204:207], v[88:91], v[116:119]\n"
    "ds_read_b64_tr_b16 v[244:245], v182 offset:12288\n"
    "s_waitcnt lgkmcnt(5)\n"
    "v_mfma_f32_16x16x32_bf16 v[116:119], v[216:219], v[92:95], v[116:119]\n"
    "ds_read_b64_tr_b16 v[246:247], v182 offset:13056\n"
    "v_mfma_f32_16x16x32_bf16 v[120:123], a[240:243], v[8:11], v[120:123]\n"
    "ds_read_b64_tr_b16 v[224:225], v183 offset:0\n"
    "v_mfma_f32_16x16x32_bf16 v[120:123], a[252:255], v[12:15], v[120:123]\n"
    "ds_read_b64_tr_b16 v[226:227], v183 offset:768\n"
    "v_mfma_f32_16x16x32_bf16 v[120:123], v[196:199], v[16:19], v[120:123]\n"
    "ds_read_b64_tr_b16 v[236:237], v183 offset:6144\n"
    "v_mfma_f32_16x16x32_bf16 v[120:123], v[208:211], v[20:23], v[120:123]\n"
    "ds_read_b64_tr_b16 v[238:239], v183 offset:6912\n"
    "v_mfma_f32_16x16x32_bf16 v[124:127], a[244:247], v[8:11], v[124:127]\n"
    "ds_read_b64_tr_b16 v[248:249], v183 offset:12288\n"
    "v_mfma_f32_16x16x32_bf16 v[124:127], v[188:191], v[12:15], v[124:127]\n"
    "ds_read_b64_tr_b16 v[250:251], v183 offset:13056\n"
    "v_mfma_f32_16x16x32_bf16 v[124:127], v[200:203], v[16:19], v[124:127]\n"
    "ds_read_b64_tr_b16 v[228:229], v182 offset:64\n"
    "v_mfma_f32_16x16x32_bf16 v[124:127], v[212:215], v[20:23], v[124:127]\n"
    "ds_read_b64_tr_b16 v[230:231], v182 offset:832\n"
    "v_mfma_f32_16x16x32_bf16 v[128:131], a[248:251], v[8:11], v[128:131]\n"
    "s_mov_b32 s26, s47\n"
    "ds_read_b64_tr_b16 v[240:241], v182 offset:6208\n"
    "v_mfma_f32_16x16x32_bf16 v[128:131], v[192:195], v[12:15], v[128:131]\n"
    "s_mov_b32 m0, s49\n"
    "ds_read_b64_tr_b16 v[242:243], v182 offset:6976\n"
    "v_mfma_f32_16x16x32_bf16 v[128:131], v[204:207], v[16:19], v[128:131]\n"
    "buffer_load_dwordx4 v174, s[24:27], s48 offen lds\n"
    "v_mfma_f32_16x16x32_bf16 v[128:131], v[216:219], v[20:23], v[128:131]\n"
    "ds_read_b64_tr_b16 v[252:253], v182 offset:12352\n"
    "s_waitcnt lgkmcnt(6)\n"
    "v_mfma_f32_16x16x32_bf16 v[108:111], v[220:223], v[96:99], v[108:111]\n"
    "s_add_u32 m0, s49, s71\n"
    "ds_read_b64_tr_b16 v[254:255], v182 offset:13120\n"
    "v_mfma_f32_16x16x32_bf16 v[108:111], v[232:235], v[100:103], v[108:111]\n"
    "buffer_load_dwordx4 v175, s[24:27], s48 offen lds\n"
    "v_mfma_f32_16x16x32_bf16 v[108:111], v[244:247], v[104:107], v[108:111]\n"
    "ds_read_b128 v[36:39], v166 offset:26112\n"
    "v_mfma_f32_16x16x32_bf16 v[112:115], v[224:227], v[96:99], v[112:115]\n"
    "ds_read_b128 v[40:43], v166 offset:26176\n"
    "v_mfma_f32_16x16x32_bf16 v[112:115], v[236:239], v[100:103], v[112:115]\n"
    "s_cmp_eq_u32 s62, 0\n"
    "s_cselect_b32 s55, s52, s53\n"
    "s_cselect_b32 s56, s52, s54\n"
    "s_waitcnt lgkmcnt(4)\n"
    "v_mfma_f32_16x16x32_bf16 v[112:115], v[248:251], v[104:107], v[112:115]\n"
    "s_add_u32 s47, s47, s55\n"
    "s_add_u32 s48, s48, s56\n"
    "s_add_u32 s49, s49, 6528\n"
    "v_mfma_f32_16x16x32_bf16 v[116:119], v[228:231], v[96:99], v[116:119]\n"
    "ds_read_b128 v[44:47], v166 offset:26240\n"
    "v_mfma_f32_16x16x32_bf16 v[116:119], v[240:243], v[100:103], v[116:119]\n"
    "ds_read_b128 v[48:51], v167 offset:26112\n"
    "s_waitcnt lgkmcnt(4)\n"
    "v_mfma_f32_16x16x32_bf16 v[116:119], v[252:255], v[104:107], v[116:119]\n"
    "ds_read_b128 v[52:55], v167 offset:26176\n"
    "v_mfma_f32_16x16x32_bf16 v[120:123], v[220:223], v[24:27], v[120:123]\n"
    "ds_read_b128 v[56:59], v167 offset:26240\n"
    "v_mfma_f32_16x16x32_bf16 v[120:123], v[232:235], v[28:31], v[120:123]\n"
    "ds_read_b128 v[60:63], v168 offset:26112\n"
    "v_cvt_pk_bf16_f32 v156, v108, v109\n"
    "v_mfma_f32_16x16x32_bf16 v[120:123], v[244:247], v[32:35], v[120:123]\n"
    "ds_read_b128 v[64:67], v168 offset:26176\n"
    "v_cvt_pk_bf16_f32 v157, v110, v111\n"
    "v_mfma_f32_16x16x32_bf16 v[124:127], v[224:227], v[24:27], v[124:127]\n"
    "ds_read_b128 v[68:71], v168 offset:26240\n"
    "v_mfma_f32_16x16x32_bf16 v[124:127], v[236:239], v[28:31], v[124:127]\n"
    "ds_write_b32 v169, v156 offset:0\n"
    "v_mfma_f32_16x16x32_bf16 v[124:127], v[248:251], v[32:35], v[124:127]\n"
    "ds_write_b32 v169, v157 offset:4\n"
    "v_mfma_f32_16x16x32_bf16 v[128:131], v[228:231], v[24:27], v[128:131]\n"
    "v_cvt_pk_bf16_f32 v158, v112, v113\n"
    "v_mfma_f32_16x16x32_bf16 v[128:131], v[240:243], v[28:31], v[128:131]\n"
    "v_cvt_pk_bf16_f32 v159, v114, v115\n"
    "v_mfma_f32_16x16x32_bf16 v[128:131], v[252:255], v[32:35], v[128:131]\n"
    "ds_write_b32 v169, v158 offset:32\n"
    "s_waitcnt lgkmcnt(5)\n"
    "v_mfma_f32_16x16x32_bf16 v[132:135], a[216:219], v[36:39], v[132:135]\n"
    "ds_write_b32 v169, v159 offset:36\n"
    "v_mfma_f32_16x16x32_bf16 v[132:135], a[228:231], v[40:43], v[132:135]\n"
    "v_cvt_pk_bf16_f32 v156, v116, v117\n"
    "v_mfma_f32_16x16x32_bf16 v[132:135], a[240:243], v[44:47], v[132:135]\n"
    "v_cvt_pk_bf16_f32 v157, v118, v119\n"
    "v_mfma_f32_16x16x32_bf16 v[132:135], a[252:255], v[48:51], v[132:135]\n"
    "ds_write_b32 v169, v156 offset:64\n"
    "v_mfma_f32_16x16x32_bf16 v[132:135], v[196:199], v[52:55], v[132:135]\n"
    "ds_write_b32 v169, v157 offset:68\n"
    "v_mfma_f32_16x16x32_bf16 v[132:135], v[208:211], v[56:59], v[132:135]\n"
    "v_mfma_f32_16x16x32_bf16 v[132:135], v[220:223], v[60:63], v[132:135]\n"
    "v_cvt_pk_bf16_f32 v158, v120, v121\n"
    "s_waitcnt lgkmcnt(3)\n"
    "v_mfma_f32_16x16x32_bf16 v[132:135], v[232:235], v[64:67], v[132:135]\n"
    "v_cvt_pk_bf16_f32 v159, v122, v123\n"
    "v_mfma_f32_16x16x32_bf16 v[132:135], v[244:247], v[68:71], v[132:135]\n"
    "ds_write_b32 v169, v158 offset:6400\n"
    "v_mfma_f32_16x16x32_bf16 v[136:139], a[220:223], v[36:39], v[136:139]\n"
    "ds_write_b32 v169, v159 offset:6404\n"
    "v_mfma_f32_16x16x32_bf16 v[136:139], a[232:235], v[40:43], v[136:139]\n"
    "v_cvt_pk_bf16_f32 v156, v124, v125\n"
    "v_mfma_f32_16x16x32_bf16 v[136:139], a[244:247], v[44:47], v[136:139]\n"
    "v_cvt_pk_bf16_f32 v157, v126, v127\n"
    "v_mfma_f32_16x16x32_bf16 v[136:139], v[188:191], v[48:51], v[136:139]\n"
    "ds_write_b32 v169, v156 offset:6432\n"
    "v_mfma_f32_16x16x32_bf16 v[136:139], v[200:203], v[52:55], v[136:139]\n"
    "ds_write_b32 v169, v157 offset:6436\n"
    "v_mfma_f32_16x16x32_bf16 v[136:139], v[212:215], v[56:59], v[136:139]\n"
    "v_cvt_pk_bf16_f32 v158, v128, v129\n"
    "v_mfma_f32_16x16x32_bf16 v[136:139], v[224:227], v[60:63], v[136:139]\n"
    "v_mov_b32 v174, v176\n"
    "v_mfma_f32_16x16x32_bf16 v[136:139], v[236:239], v[64:67], v[136:139]\n"
    "s_mov_b32 s26, s47\n"
    "v_cvt_pk_bf16_f32 v159, v130, v131\n"
    "v_mfma_f32_16x16x32_bf16 v[136:139], v[248:251], v[68:71], v[136:139]\n"
    "s_mov_b32 m0, s49\n"
    "ds_write_b32 v169, v158 offset:6464\n"
    "v_mfma_f32_16x16x32_bf16 v[140:143], a[224:227], v[36:39], v[140:143]\n"
    "buffer_load_dwordx4 v174, s[24:27], s48 offen lds\n"
    "v_mfma_f32_16x16x32_bf16 v[140:143], a[236:239], v[40:43], v[140:143]\n"
    "s_add_u32 m0, s49, s71\n"
    "ds_write_b32 v169, v159 offset:6468\n"
    "v_mfma_f32_16x16x32_bf16 v[140:143], a[248:251], v[44:47], v[140:143]\n"
    "buffer_load_dwordx4 v175, s[24:27], s48 offen lds\n"
    "v_cvt_pk_bf16_f32 v156, v132, v133\n"
    "v_mfma_f32_16x16x32_bf16 v[140:143], v[192:195], v[48:51], v[140:143]\n"
    "v_cvt_pk_bf16_f32 v157, v134, v135\n"
    "v_mfma_f32_16x16x32_bf16 v[140:143], v[204:207], v[52:55], v[140:143]\n"
    "ds_write_b32 v169, v156 offset:12800\n"
    "v_mfma_f32_16x16x32_bf16 v[140:143], v[216:219], v[56:59], v[140:143]\n"
    "s_add_u32 s47, s47, s52\n"
    "s_add_u32 s48, s48, s52\n"
    "s_add_u32 s49, s49, 6528\n"
    "v_mfma_f32_16x16x32_bf16 v[140:143], v[228:231], v[60:63], v[140:143]\n"
    "ds_write_b32 v169, v157 offset:12804\n"
    "v_mfma_f32_16x16x32_bf16 v[140:143], v[240:243], v[64:67], v[140:143]\n"
    "v_mfma_f32_16x16x32_bf16 v[140:143], v[252:255], v[68:71], v[140:143]\n"
    "s_waitcnt lgkmcnt(8)\n"
    "s_waitcnt vmcnt(4)\n"
    "v_cvt_pk_bf16_f32 v158, v136, v137\n"
    "v_mfma_f32_16x16x32_bf16 v[108:111], a[0:3], v[0:3], v[144:147]\n"
    "s_barrier\n"
    "v_mfma_f32_16x16x32_bf16 v[108:111], a[12:15], v[4:7], v[108:111]\n"
    "ds_read_b128 v[72:75], v166 offset:32640\n"
    "v_cvt_pk_bf16_f32 v159, v138, v139\n"
    "v_mfma_f32_16x16x32_bf16 v[108:111], a[24:27], v[8:11], v[108:111]\n"
    "ds_read_b32 v160, v170 offset:0\n"
    "ds_read_b32 v161, v171 offset:0\n"
    "v_mfma_f32_16x16x32_bf16 v[108:111], a[36:39], v[12:15], v[108:111]\n"
    "ds_read_b32 v162, v172 offset:0\n"
    "ds_read_b32 v163, v170 offset:800\n"
    "v_mfma_f32_16x16x32_bf16 v[108:111], a[48:51], v[16:19], v[108:111]\n"
    "ds_read_b32 v164, v171 offset:800\n"
    "ds_read_b32 v165, v172 offset:800\n"
    "v_mfma_f32_16x16x32_bf16 v[108:111], a[60:63], v[20:23], v[108:111]\n"
    "ds_read_b128 v[76:79], v166 offset:32704\n"
    "v_mfma_f32_16x16x32_bf16 v[108:111], a[72:75], v[24:27], v[108:111]\n"
    "ds_read_b128 v[80:83], v166 offset:32768\n"
    "v_mfma_f32_16x16x32_bf16 v[108:111], a[84:87], v[28:31], v[108:111]\n"
    "ds_read_b128 v[84:87], v167 offset:32640\n"
    "v_mfma_f32_16x16x32_bf16 v[108:111], a[96:99], v[32:35], v[108:111]\n"
    "ds_read_b128 v[88:91], v167 offset:32704\n"
    "v_mfma_f32_16x16x32_bf16 v[108:111], a[108:111], v[36:39], v[108:111]\n"
    "ds_read_b128 v[92:95], v167 offset:32768\n"
    "v_mfma_f32_16x16x32_bf16 v[108:111], a[120:123], v[40:43], v[108:111]\n"
    "ds_read_b128 v[96:99], v168 offset:32640\n"
    "v_mfma_f32_16x16x32_bf16 v[108:111], a[132:135], v[44:47], v[108:111]\n"
    "ds_read_b128 v[100:103], v168 offset:32704\n"
    "v_mfma_f32_16x16x32_bf16 v[108:111], a[144:147], v[48:51], v[108:111]\n"
    "ds_read_b128 v[104:107], v168 offset:32768\n"
    "v_mfma_f32_16x16x32_bf16 v[108:111], a[156:159], v[52:55], v[108:111]\n"
    "s_waitcnt lgkmcnt(6)\n"
    "buffer_store_dword v160, v173, s[36:39], s59 offen offset:0 sc1\n"
    "v_mfma_f32_16x16x32_bf16 v[108:111], a[168:171], v[56:59], v[108:111]\n"
    "ds_write_b32 v169, v158 offset:12832\n"
    "v_mfma_f32_16x16x32_bf16 v[108:111], a[180:183], v[60:63], v[108:111]\n"
    "buffer_store_dword v161, v173, s[36:39], s59 offen offset:256 sc1\n"
    "v_mfma_f32_16x16x32_bf16 v[108:111], a[192:195], v[64:67], v[108:111]\n"
    "ds_write_b32 v169, v159 offset:12836\n"
    "v_mfma_f32_16x16x32_bf16 v[108:111], a[204:207], v[68:71], v[108:111]\n"
    "buffer_store_dword v162, v173, s[36:39], s59 offen offset:512 sc1\n"
    "v_cvt_pk_bf16_f32 v156, v140, v141\n"
    "v_mfma_f32_16x16x32_bf16 v[108:111], a[216:219], v[72:75], v[108:111]\n"
    "s_mov_b32 s26, s47\n"
    "v_cvt_pk_bf16_f32 v157, v142, v143\n"
    "v_mfma_f32_16x16x32_bf16 v[108:111], a[228:231], v[76:79], v[108:111]\n"
    "buffer_store_dword v163, v173, s[36:39], s59 offen offset:768 sc1\n"
    "s_mov_b32 m0, s49\n"
    "v_mfma_f32_16x16x32_bf16 v[108:111], a[240:243], v[80:83], v[108:111]\n"
    "ds_write_b32 v169, v156 offset:12864\n"
    "s_waitcnt lgkmcnt(3)\n"
    "v_mfma_f32_16x16x32_bf16 v[108:111], a[252:255], v[84:87], v[108:111]\n"
    "buffer_store_dword v164, v173, s[36:39], s59 offen offset:1024 sc1\n"
    "v_mfma_f32_16x16x32_bf16 v[108:111], v[196:199], v[88:91], v[108:111]\n"
    "ds_write_b32 v169, v157 offset:12868\n"
    "v_mfma_f32_16x16x32_bf16 v[108:111], v[208:211], v[92:95], v[108:111]\n"
    "buffer_store_dword v165, v173, s[36:39], s59 offen offset:1280 sc1\n"
    "v_mfma_f32_16x16x32_bf16 v[108:111], v[220:223], v[96:99], v[108:111]\n"
    "s_add_u32 s59, s59, s52\n"
    "v_mfma_f32_16x16x32_bf16 v[108:111], v[232:235], v[100:103], v[108:111]\n"
    "buffer_load_dwordx4 v174, s[24:27], s48 offen lds\n"
    "v_mfma_f32_16x16x32_bf16 v[108:111], v[244:247], v[104:107], v[108:111]\n"
    "s_add_u32 m0, s49, s71\n"
    "v_add_u32 v166, 39168, v166\n"
    "v_mfma_f32_16x16x32_bf16 v[112:115], a[4:7], v[0:3], v[148:151]\n"
    "buffer_load_dwordx4 v175, s[24:27], s48 offen lds\n"
    "v_add_u32 v167, 39168, v167\n"
    "v_mfma_f32_16x16x32_bf16 v[112:115], a[16:19], v[4:7], v[112:115]\n"
    "s_add_u32 s47, s47, s52\n"
    "s_add_u32 s48, s48, s52\n"
    "s_add_u32 s49, s49, 6528\n"
    "v_mfma_f32_16x16x32_bf16 v[112:115], a[28:31], v[8:11], v[112:115]\n"
    "v_add_u32 v168, 39168, v168\n"
    "v_mfma_f32_16x16x32_bf16 v[112:115], a[40:43], v[12:15], v[112:115]\n"
    "v_mfma_f32_16x16x32_bf16 v[112:115], a[52:55], v[16:19], v[112:115]\n"
    "v_mfma_f32_16x16x32_bf16 v[112:115], a[64:67], v[20:23], v[112:115]\n"
    "v_mfma_f32_16x16x32_bf16 v[112:115], a[76:79], v[24:27], v[112:115]\n"
    "v_mfma_f32_16x16x32_bf16 v[112:115], a[88:91], v[28:31], v[112:115]\n"
    "v_mfma_f32_16x16x32_bf16 v[112:115], a[100:103], v[32:35], v[112:115]\n"
    "s_waitcnt lgkmcnt(0)\n"
    "s_waitcnt vmcnt(10)\n"
    "v_mfma_f32_16x16x32_bf16 v[112:115], a[112:115], v[36:39], v[112:115]\n"
    "s_barrier\n"
    "v_mfma_f32_16x16x32_bf16 v[112:115], a[124:127], v[40:43], v[112:115]\n"
    "ds_read_b32 v160, v170 offset:6400\n"
    "v_cvt_pk_bf16_f32 v158, v108, v109\n"
    "v_mfma_f32_16x16x32_bf16 v[112:115], a[136:139], v[44:47], v[112:115]\n"
    "s_cmp_lg_u32 s62, 0\n"
    "s_cbranch_scc1 .Lconv_extra%=\n"
    ".Lconv_extraret%=:\n"
    "v_mfma_f32_16x16x32_bf16 v[112:115], a[148:151], v[48:51], v[112:115]\n"
    "ds_read_b32 v161, v171 offset:6400\n"
    "v_cvt_pk_bf16_f32 v159, v110, v111\n"
    "v_mfma_f32_16x16x32_bf16 v[112:115], a[160:163], v[52:55], v[112:115]\n"
    "ds_read_b32 v162, v172 offset:6400\n"
    "v_mfma_f32_16x16x32_bf16 v[112:115], a[172:175], v[56:59], v[112:115]\n"
    "ds_read_b32 v163, v170 offset:7200\n"
    "v_mfma_f32_16x16x32_bf16 v[112:115], a[184:187], v[60:63], v[112:115]\n"
    "ds_read_b32 v164, v171 offset:7200\n"
    "v_mfma_f32_16x16x32_bf16 v[112:115], a[196:199], v[64:67], v[112:115]\n"
    "ds_read_b32 v165, v172 offset:7200\n"
    "v_mfma_f32_16x16x32_bf16 v[112:115], a[208:211], v[68:71], v[112:115]\n"
    "ds_read_b32 v120, v170 offset:12800\n"
    "v_mfma_f32_16x16x32_bf16 v[112:115], a[220:223], v[72:75], v[112:115]\n"
    "ds_read_b32 v121, v171 offset:12800\n"
    "v_mfma_f32_16x16x32_bf16 v[112:115], a[232:235], v[76:79], v[112:115]\n"
    "ds_read_b32 v122, v172 offset:12800\n"
    "v_mfma_f32_16x16x32_bf16 v[112:115], a[244:247], v[80:83], v[112:115]\n"
    "ds_read_b32 v123, v170 offset:13600\n"
    "v_mfma_f32_16x16x32_bf16 v[112:115], v[188:191], v[84:87], v[112:115]\n"
    "ds_read_b32 v124, v171 offset:13600\n"
    "v_mfma_f32_16x16x32_bf16 v[112:115], v[200:203], v[88:91], v[112:115]\n"
    "ds_read_b32 v125, v172 offset:13600\n"
    "v_mfma_f32_16x16x32_bf16 v[112:115], v[212:215], v[92:95], v[112:115]\n"
    "s_waitcnt lgkmcnt(6)\n"
    "buffer_store_dword v160, v173, s[36:39], s59 offen offset:0 sc1\n"
    "v_mfma_f32_16x16x32_bf16 v[112:115], v[224:227], v[96:99], v[112:115]\n"
    "ds_write_b32 v169, v158 offset:0\n"
    "v_mfma_f32_16x16x32_bf16 v[112:115], v[236:239], v[100:103], v[112:115]\n"
    "buffer_store_dword v161, v173, s[36:39], s59 offen offset:256 sc1\n"
    "v_mfma_f32_16x16x32_bf16 v[112:115], v[248:251], v[104:107], v[112:115]\n"
    "ds_write_b32 v169, v159 offset:4\n"
    "v_mfma_f32_16x16x32_bf16 v[116:119], a[8:11], v[0:3], v[152:155]\n"
    "buffer_store_dword v162, v173, s[36:39], s59 offen offset:512 sc1\n"
    "s_mov_b32 s26, s47\n"
    "v_mfma_f32_16x16x32_bf16 v[116:119], a[20:23], v[4:7], v[116:119]\n"
    "s_mov_b32 m0, s49\n"
    "ds_read_b128 v[0:3], v166 offset:0\n"
    "v_mfma_f32_16x16x32_bf16 v[116:119], a[32:35], v[8:11], v[116:119]\n"
    "buffer_load_dwordx4 v174, s[24:27], s48 offen lds\n"
    "v_mfma_f32_16x16x32_bf16 v[116:119], a[44:47], v[12:15], v[116:119]\n"
    "ds_read_b128 v[4:7], v166 offset:64\n"
    "v_mfma_f32_16x16x32_bf16 v[116:119], a[56:59], v[16:19], v[116:119]\n"
    "buffer_store_dword v163, v173, s[36:39], s59 offen offset:768 sc1\n"
    "s_add_u32 m0, s49, s71\n"
    "v_mfma_f32_16x16x32_bf16 v[116:119], a[68:71], v[20:23], v[116:119]\n"
    "ds_read_b128 v[8:11], v166 offset:128\n"
    "v_mfma_f32_16x16x32_bf16 v[116:119], a[80:83], v[24:27], v[116:119]\n"
    "buffer_load_dwordx4 v175, s[24:27], s48 offen lds\n"
    "v_mfma_f32_16x16x32_bf16 v[116:119], a[92:95], v[28:31], v[116:119]\n"
    "ds_read_b128 v[12:15], v167 offset:0\n"
    "v_mfma_f32_16x16x32_bf16 v[116:119], a[104:107], v[32:35], v[116:119]\n"
    "s_add_u32 s47, s47, s52\n"
    "s_add_u32 s48, s48, s52\n"
    "s_add_u32 s49, s49, 6528\n"
    "v_mfma_f32_16x16x32_bf16 v[116:119], a[116:119], v[36:39], v[116:119]\n"
    "buffer_store_dword v164, v173, s[36:39], s59 offen offset:1024 sc1\n"
    "v_cvt_pk_bf16_f32 v156, v112, v113\n"
    "v_mfma_f32_16x16x32_bf16 v[116:119], a[128:131], v[40:43], v[116:119]\n"
    "ds_read_b128 v[16:19], v167 offset:64\n"
    "v_cvt_pk_bf16_f32 v157, v114, v115\n"
    "v_mfma_f32_16x16x32_bf16 v[116:119], a[140:143], v[44:47], v[116:119]\n"
    "buffer_store_dword v165, v173, s[36:39], s59 offen offset:1280 sc1\n"
    "v_mfma_f32_16x16x32_bf16 v[116:119], a[152:155], v[48:51], v[116:119]\n"
    "s_add_u32 s59, s59, s52\n"
    "ds_read_b128 v[20:23], v167 offset:128\n"
    "v_mfma_f32_16x16x32_bf16 v[116:119], a[164:167], v[52:55], v[116:119]\n"
    "s_waitcnt lgkmcnt(3)\n"
    "buffer_store_dword v120, v173, s[36:39], s59 offen offset:0 sc1\n"
    "v_mfma_f32_16x16x32_bf16 v[116:119], a[176:179], v[56:59], v[116:119]\n"
    "ds_read_b128 v[24:27], v168 offset:0\n"
    "v_mfma_f32_16x16x32_bf16 v[116:119], a[188:191], v[60:63], v[116:119]\n"
    "buffer_store_dword v121, v173, s[36:39], s59 offen offset:256 sc1\n"
    "v_mfma_f32_16x16x32_bf16 v[116:119], a[200:203], v[64:67], v[116:119]\n"
    "ds_read_b128 v[28:31], v168 offset:64\n"
    "v_mfma_f32_16x16x32_bf16 v[116:119], a[212:215], v[68:71], v[116:119]\n"
    "buffer_store_dword v122, v173, s[36:39], s59 offen offset:512 sc1\n"
    "v_mfma_f32_16x16x32_bf16 v[116:119], a[224:227], v[72:75], v[116:119]\n"
    "ds_read_b128 v[32:35], v168 offset:128\n"
    "v_mfma_f32_16x16x32_bf16 v[116:119], a[236:239], v[76:79], v[116:119]\n"
    "buffer_store_dword v123, v173, s[36:39], s59 offen offset:768 sc1\n"
    "v_mfma_f32_16x16x32_bf16 v[116:119], a[248:251], v[80:83], v[116:119]\n"
    "ds_write_b32 v169, v156 offset:32\n"
    "v_mfma_f32_16x16x32_bf16 v[116:119], v[192:195], v[84:87], v[116:119]\n"
    "buffer_store_dword v124, v173, s[36:39], s59 offen offset:1024 sc1\n"
    "v_mfma_f32_16x16x32_bf16 v[116:119], v[204:207], v[88:91], v[116:119]\n"
    "ds_write_b32 v169, v157 offset:36\n"
    "v_mfma_f32_16x16x32_bf16 v[116:119], v[216:219], v[92:95], v[116:119]\n"
    "buffer_store_dword v125, v173, s[36:39], s59 offen offset:1280 sc1\n"
    "v_mfma_f32_16x16x32_bf16 v[116:119], v[228:231], v[96:99], v[116:119]\n"
    "s_add_u32 s59, s59, s52\n"
    "v_mfma_f32_16x16x32_bf16 v[116:119], v[240:243], v[100:103], v[116:119]\n"
    "v_mfma_f32_16x16x32_bf16 v[116:119], v[252:255], v[104:107], v[116:119]\n"
    ".Lconv_r1%=:\n"
    "v_mfma_f32_16x16x32_bf16 v[108:111], a[0:3], v[36:39], v[144:147]\n"
    "s_sub_u32 s55, s47, 73920\n"
    "s_cmp_lt_u32 s55, 28311552\n"
    "s_cselect_b32 s26, s47, 0\n"
    "v_mfma_f32_16x16x32_bf16 v[108:111], a[12:15], v[40:43], v[108:111]\n"
    "s_mov_b32 m0, s49\n"
    "v_mfma_f32_16x16x32_bf16 v[108:111], a[24:27], v[44:47], v[108:111]\n"
    "buffer_load_dwordx4 v174, s[24:27], s48 offen lds\n"
    "v_mfma_f32_16x16x32_bf16 v[108:111], a[36:39], v[48:51], v[108:111]\n"
    "v_mfma_f32_16x16x32_bf16 v[108:111], a[48:51], v[52:55], v[108:111]\n"
    "v_mfma_f32_16x16x32_bf16 v[108:111], a[60:63], v[56:59], v[108:111]\n"
    "s_add_u32 m0, s49, s71\n"
    "v_mfma_f32_16x16x32_bf16 v[108:111], a[72:75], v[60:63], v[108:111]\n"
    "buffer_load_dwordx4 v175, s[24:27], s48 offen lds\n"
    "v_mfma_f32_16x16x32_bf16 v[108:111], a[84:87], v[64:67], v[108:111]\n"
    "v_mfma_f32_16x16x32_bf16 v[108:111], a[96:99], v[68:71], v[108:111]\n"
    "s_waitcnt lgkmcnt(0)\n"
    "v_mfma_f32_16x16x32_bf16 v[108:111], a[108:111], v[72:75], v[108:111]\n"
    "v_cvt_pk_bf16_f32 v156, v116, v117\n"
    "v_mfma_f32_16x16x32_bf16 v[108:111], a[120:123], v[76:79], v[108:111]\n"
    "v_cvt_pk_bf16_f32 v157, v118, v119\n"
    "v_mfma_f32_16x16x32_bf16 v[108:111], a[132:135], v[80:83], v[108:111]\n"
    "ds_write_b32 v169, v156 offset:64\n"
    "v_mfma_f32_16x16x32_bf16 v[108:111], a[144:147], v[84:87], v[108:111]\n"
    "ds_write_b32 v169, v157 offset:68\n"
    "v_mfma_f32_16x16x32_bf16 v[108:111], a[156:159], v[88:91], v[108:111]\n"
    "v_mfma_f32_16x16x32_bf16 v[108:111], a[168:171], v[92:95], v[108:111]\n"
    "v_mfma_f32_16x16x32_bf16 v[108:111], a[180:183], v[96:99], v[108:111]\n"
    "v_mfma_f32_16x16x32_bf16 v[108:111], a[192:195], v[100:103], v[108:111]\n"
    "v_mfma_f32_16x16x32_bf16 v[108:111], a[204:207], v[104:107], v[108:111]\n"
    "s_waitcnt lgkmcnt(0)\n"
    "v_mfma_f32_16x16x32_bf16 v[108:111], a[216:219], v[0:3], v[108:111]\n"
    "s_waitcnt vmcnt(24)\n"
    "v_mfma_f32_16x16x32_bf16 v[108:111], a[228:231], v[4:7], v[108:111]\n"
    "s_barrier\n"
    "v_mfma_f32_16x16x32_bf16 v[108:111], a[240:243], v[8:11], v[108:111]\n"
    "ds_read_b32 v160, v170 offset:0\n"
    "v_mfma_f32_16x16x32_bf16 v[108:111], a[252:255], v[12:15], v[108:111]\n"
    "ds_read_b32 v161, v171 offset:0\n"
    "v_mfma_f32_16x16x32_bf16 v[108:111], v[196:199], v[16:19], v[108:111]\n"
    "ds_read_b32 v162, v172 offset:0\n"
    "v_mfma_f32_16x16x32_bf16 v[108:111], v[208:211], v[20:23], v[108:111]\n"
    "ds_read_b32 v163, v170 offset:800\n"
    "v_mfma_f32_16x16x32_bf16 v[108:111], v[220:223], v[24:27], v[108:111]\n"
    "ds_read_b32 v164, v171 offset:800\n"
    "v_mfma_f32_16x16x32_bf16 v[108:111], v[232:235], v[28:31], v[108:111]\n"
    "ds_read_b32 v165, v172 offset:800\n"
    "v_mfma_f32_16x16x32_bf16 v[108:111], v[244:247], v[32:35], v[108:111]\n"
    "s_sub_u32 s44, s44, 1\n"
    "v_mfma_f32_16x16x32_bf16 v[112:115], a[4:7], v[36:39], v[148:151]\n"
    "s_cmp_lg_u32 s44, 0\n"
    "s_cselect_b32 s55, s52, s53\n"
    "s_cselect_b32 s56, s52, s54\n"
    "v_mfma_f32_16x16x32_bf16 v[112:115], a[16:19], v[40:43], v[112:115]\n"
    "s_cmp_lg_u32 s44, 0\n"
    "s_cselect_b32 s44, s44, 7\n"
    "v_mfma_f32_16x16x32_bf16 v[112:115], a[28:31], v[44:47], v[112:115]\n"
    "s_waitcnt lgkmcnt(0)\n"
    "v_mfma_f32_16x16x32_bf16 v[112:115], a[40:43], v[48:51], v[112:115]\n"
    "buffer_store_dword v160, v173, s[36:39], s59 offen offset:0 sc1\n"
    "v_mfma_f32_16x16x32_bf16 v[112:115], a[52:55], v[52:55], v[112:115]\n"
    "s_add_u32 s47, s47, s55\n"
    "s_add_u32 s48, s48, s56\n"
    "v_mfma_f32_16x16x32_bf16 v[112:115], a[64:67], v[56:59], v[112:115]\n"
    "s_add_u32 s49, s49, 6528\n"
    "s_min_u32 s49, s49, s50\n"
    ".Lconv_pad1%=:\n"
    "s_cbranch_execz .Lconv_back%=\n"
    "v_mfma_f32_16x16x32_bf16 v[112:115], a[76:79], v[60:63], v[112:115]\n"
    "buffer_store_dword v161, v173, s[36:39], s59 offen offset:256 sc1\n"
    "v_mfma_f32_16x16x32_bf16 v[112:115], a[88:91], v[64:67], v[112:115]\n"
    "v_mfma_f32_16x16x32_bf16 v[112:115], a[100:103], v[68:71], v[112:115]\n"
    "v_cvt_pk_bf16_f32 v158, v108, v109\n"
    "v_mfma_f32_16x16x32_bf16 v[112:115], a[112:115], v[72:75], v[112:115]\n"
    "buffer_store_dword v162, v173, s[36:39], s59 offen offset:512 sc1\n"
    "v_mfma_f32_16x16x32_bf16 v[112:115], a[124:127], v[76:79], v[112:115]\n"
    "v_cvt_pk_bf16_f32 v159, v110, v111\n"
    "v_mfma_f32_16x16x32_bf16 v[112:115], a[136:139], v[80:83], v[112:115]\n"
    "ds_write_b32 v169, v158 offset:6400\n"
    "v_mfma_f32_16x16x32_bf16 v[112:115], a[148:151], v[84:87], v[112:115]\n"
    "buffer_store_dword v163, v173, s[36:39], s59 offen offset:768 sc1\n"
    "v_mfma_f32_16x16x32_bf16 v[112:115], a[160:163], v[88:91], v[112:115]\n"
    "ds_write_b32 v169, v159 offset:6404\n"
    "v_mfma_f32_16x16x32_bf16 v[112:115], a[172:175], v[92:95], v[112:115]\n"
    "v_mfma_f32_16x16x32_bf16 v[112:115], a[184:187], v[96:99], v[112:115]\n"
    "buffer_store_dword v164, v173, s[36:39], s59 offen offset:1024 sc1\n"
    "v_mfma_f32_16x16x32_bf16 v[112:115], a[196:199], v[100:103], v[112:115]\n"
    "v_mfma_f32_16x16x32_bf16 v[112:115], a[208:211], v[104:107], v[112:115]\n"
    "v_mfma_f32_16x16x32_bf16 v[112:115], a[220:223], v[0:3], v[112:115]\n"
    "buffer_store_dword v165, v173, s[36:39], s59 offen offset:1280 sc1\n"
    "v_mfma_f32_16x16x32_bf16 v[112:115], a[232:235], v[4:7], v[112:115]\n"
    "v_mfma_f32_16x16x32_bf16 v[112:115], a[244:247], v[8:11], v[112:115]\n"
    "v_mfma_f32_16x16x32_bf16 v[112:115], v[188:191], v[12:15], v[112:115]\n"
    "v_mfma_f32_16x16x32_bf16 v[112:115], v[200:203], v[16:19], v[112:115]\n"
    "v_mfma_f32_16x16x32_bf16 v[112:115], v[212:215], v[20:23], v[112:115]\n"
    "v_mfma_f32_16x16x32_bf16 v[112:115], v[224:227], v[24:27], v[112:115]\n"
    "v_mfma_f32_16x16x32_bf16 v[112:115], v[236:239], v[28:31], v[112:115]\n"
    "s_add_u32 s59, s59, s52\n"
    "v_mfma_f32_16x16x32_bf16 v[112:115], v[248:251], v[32:35], v[112:115]\n"
    "v_mfma_f32_16x16x32_bf16 v[116:119], a[8:11], v[36:39], v[152:155]\n"
    "v_mfma_f32_16x16x32_bf16 v[116:119], a[20:23], v[40:43], v[116:119]\n"
    "ds_read_b128 v[36:39], v166 offset:6528\n"
    "v_mfma_f32_16x16x32_bf16 v[116:119], a[32:35], v[44:47], v[116:119]\n"
    "v_mfma_f32_16x16x32_bf16 v[116:119], a[44:47], v[48:51], v[116:119]\n"
    "ds_read_b128 v[40:43], v166 offset:6592\n"
    "v_mfma_f32_16x16x32_bf16 v[116:119], a[56:59], v[52:55], v[116:119]\n"
    "v_mfma_f32_16x16x32_bf16 v[116:119], a[68:71], v[56:59], v[116:119]\n"
    "ds_read_b128 v[44:47], v166 offset:6656\n"
    "v_mfma_f32_16x16x32_bf16 v[116:119], a[80:83], v[60:63], v[116:119]\n"
    "v_mfma_f32_16x16x32_bf16 v[116:119], a[92:95], v[64:67], v[116:119]\n"
    "ds_read_b128 v[48:51], v167 offset:6528\n"
    "v_mfma_f32_16x16x32_bf16 v[116:119], a[104:107], v[68:71], v[116:119]\n"
    "v_cvt_pk_bf16_f32 v156, v112, v113\n"
    "v_mfma_f32_16x16x32_bf16 v[116:119], a[116:119], v[72:75], v[116:119]\n"
    "v_cvt_pk_bf16_f32 v157, v114, v115\n"
    "v_mfma_f32_16x16x32_bf16 v[116:119], a[128:131], v[76:79], v[116:119]\n"
    "ds_write_b32 v169, v156 offset:6432\n"
    "v_mfma_f32_16x16x32_bf16 v[116:119], a[140:143], v[80:83], v[116:119]\n"
    "ds_write_b32 v169, v157 offset:6436\n"
    "v_mfma_f32_16x16x32_bf16 v[116:119], a[152:155], v[84:87], v[116:119]\n"
    "v_mfma_f32_16x16x32_bf16 v[116:119], a[164:167], v[88:91], v[116:119]\n"
    "ds_read_b128 v[52:55], v167 offset:6592\n"
    "v_mfma_f32_16x16x32_bf16 v[116:119], a[176:179], v[92:95], v[116:119]\n"
    "v_mfma_f32_16x16x32_bf16 v[116:119], a[188:191], v[96:99], v[116:119]\n"
    "ds_read_b128 v[56:59], v167 offset:6656\n"
    "v_mfma_f32_16x16x32_bf16 v[116:119], a[200:203], v[100:103], v[116:119]\n"
    "v_mfma_f32_16x16x32_bf16 v[116:119], a[212:215], v[104:107], v[116:119]\n"
    "ds_read_b128 v[60:63], v168 offset:6528\n"
    "v_mfma_f32_16x16x32_bf16 v[116:119], a[224:227], v[0:3], v[116:119]\n"
    "v_mfma_f32_16x16x32_bf16 v[116:119], a[236:239], v[4:7], v[116:119]\n"
    "ds_read_b128 v[64:67], v168 offset:6592\n"
    "v_mfma_f32_16x16x32_bf16 v[116:119], a[248:251], v[8:11], v[116:119]\n"
    "v_mfma_f32_16x16x32_bf16 v[116:119], v[192:195], v[12:15], v[116:119]\n"
    "ds_read_b128 v[68:71], v168 offset:6656\n"
    "v_mfma_f32_16x16x32_bf16 v[116:119], v[204:207], v[16:19], v[116:119]\n"
    "v_mfma_f32_16x16x32_bf16 v[116:119], v[216:219], v[20:23], v[116:119]\n"
    "v_mfma_f32_16x16x32_bf16 v[116:119], v[228:231], v[24:27], v[116:119]\n"
    "s_bitcmp1_b32 s62, s61\n"
    "v_mfma_f32_16x16x32_bf16 v[116:119], v[240:243], v[28:31], v[116:119]\n"
    "v_mfma_f32_16x16x32_bf16 v[116:119], v[252:255], v[32:35], v[116:119]\n"
    "s_cbranch_scc1 .Lconv_switch%=\n"
    "v_mfma_f32_16x16x32_bf16 v[108:111], a[0:3], v[72:75], v[144:147]\n"
    "s_sub_u32 s55, s47, 73920\n"
    "s_cmp_lt_u32 s55, 28311552\n"
    "s_cselect_b32 s26, s47, 0\n"
    "v_mfma_f32_16x16x32_bf16 v[108:111], a[12:15], v[76:79], v[108:111]\n"
    "s_mov_b32 m0, s49\n"
    "v_mfma_f32_16x16x32_bf16 v[108:111], a[24:27], v[80:83], v[108:111]\n"
    "buffer_load_dwordx4 v174, s[24:27], s48 offen lds\n"
    "v_mfma_f32_16x16x32_bf16 v[108:111], a[36:39], v[84:87], v[108:111]\n"
    "v_mfma_f32_16x16x32_bf16 v[108:111], a[48:51], v[88:91], v[108:111]\n"
    "v_mfma_f32_16x16x32_bf16 v[108:111], a[60:63], v[92:95], v[108:111]\n"
    "s_add_u32 m0, s49, s71\n"
    "v_mfma_f32_16x16x32_bf16 v[108:111], a[72:75], v[96:99], v[108:111]\n"
    "buffer_load_dwordx4 v175, s[24:27], s48 offen lds\n"
    "v_mfma_f32_16x16x32_bf16 v[108:111], a[84:87], v[100:103], v[108:111]\n"
    "v_mfma_f32_16x16x32_bf16 v[108:111], a[96:99], v[104:107], v[108:111]\n"
    "s_waitcnt lgkmcnt(0)\n"
    "v_mfma_f32_16x16x32_bf16 v[108:111], a[108:111], v[0:3], v[108:111]\n"
    "v_cvt_pk_bf16_f32 v156, v116, v117\n"
    "v_mfma_f32_16x16x32_bf16 v[108:111], a[120:123], v[4:7], v[108:111]\n"
    "v_cvt_pk_bf16_f32 v157, v118, v119\n"
    "v_mfma_f32_16x16x32_bf16 v[108:111], a[132:135], v[8:11], v[108:111]\n"
    "ds_write_b32 v169, v156 offset:6464\n"
    "v_mfma_f32_16x16x32_bf16 v[108:111], a[144:147], v[12:15], v[108:111]\n"
    "ds_write_b32 v169, v157 offset:6468\n"
    "v_mfma_f32_16x16x32_bf16 v[108:111], a[156:159], v[16:19], v[108:111]\n"
    "v_mfma_f32_16x16x32_bf16 v[108:111], a[168:171], v[20:23], v[108:111]\n"
    "v_mfma_f32_16x16x32_bf16 v[108:111], a[180:183], v[24:27], v[108:111]\n"
    "v_mfma_f32_16x16x32_bf16 v[108:111], a[192:195], v[28:31], v[108:111]\n"
    "v_mfma_f32_16x16x32_bf16 v[108:111], a[204:207], v[32:35], v[108:111]\n"
    "s_waitcnt lgkmcnt(0)\n"
    "v_mfma_f32_16x16x32_bf16 v[108:111], a[216:219], v[36:39], v[108:111]\n"
    "s_waitcnt vmcnt(24)\n"
    "v_mfma_f32_16x16x32_bf16 v[108:111], a[228:231], v[40:43], v[108:111]\n"
    "s_barrier\n"
    "v_mfma_f32_16x16x32_bf16 v[108:111], a[240:243], v[44:47], v[108:111]\n"
    "ds_read_b32 v160, v170 offset:6400\n"
    "v_mfma_f32_16x16x32_bf16 v[108:111], a[252:255], v[48:51], v[108:111]\n"
    "ds_read_b32 v161, v171 offset:6400\n"
    "v_mfma_f32_16x16x32_bf16 v[108:111], v[196:199], v[52:55], v[108:111]\n"
    "ds_read_b32 v162, v172 offset:6400\n"
    "v_mfma_f32_16x16x32_bf16 v[108:111], v[208:211], v[56:59], v[108:111]\n"
    "ds_read_b32 v163, v170 offset:7200\n"
    "v_mfma_f32_16x16x32_bf16 v[108:111], v[220:223], v[60:63], v[108:111]\n"
    "ds_read_b32 v164, v171 offset:7200\n"
    "v_mfma_f32_16x16x32_bf16 v[108:111], v[232:235], v[64:67], v[108:111]\n"
    "ds_read_b32 v165, v172 offset:7200\n"
    "v_mfma_f32_16x16x32_bf16 v[108:111], v[244:247], v[68:71], v[108:111]\n"
    "s_sub_u32 s44, s44, 1\n"
    "v_mfma_f32_16x16x32_bf16 v[112:115], a[4:7], v[72:75], v[148:151]\n"
    "s_cmp_lg_u32 s44, 0\n"
    "s_cselect_b32 s55, s52, s53\n"
    "s_cselect_b32 s56, s52, s54\n"
    "v_mfma_f32_16x16x32_bf16 v[112:115], a[16:19], v[76:79], v[112:115]\n"
    "s_cmp_lg_u32 s44, 0\n"
    "s_cselect_b32 s44, s44, 7\n"
    "v_mfma_f32_16x16x32_bf16 v[112:115], a[28:31], v[80:83], v[112:115]\n"
    "s_waitcnt lgkmcnt(0)\n"
    "v_mfma_f32_16x16x32_bf16 v[112:115], a[40:43], v[84:87], v[112:115]\n"
    "buffer_store_dword v160, v173, s[36:39], s59 offen offset:0 sc1\n"
    "v_mfma_f32_16x16x32_bf16 v[112:115], a[52:55], v[88:91], v[112:115]\n"
    "s_add_u32 s47, s47, s55\n"
    "s_add_u32 s48, s48, s56\n"
    "v_mfma_f32_16x16x32_bf16 v[112:115], a[64:67], v[92:95], v[112:115]\n"
    "s_add_u32 s49, s49, 6528\n"
    "s_min_u32 s49, s49, s50\n"
    "v_mfma_f32_16x16x32_bf16 v[112:115], a[76:79], v[96:99], v[112:115]\n"
    "buffer_store_dword v161, v173, s[36:39], s59 offen offset:256 sc1\n"
    "v_mfma_f32_16x16x32_bf16 v[112:115], a[88:91], v[100:103], v[112:115]\n"
    "v_mfma_f32_16x16x32_bf16 v[112:115], a[100:103], v[104:107], v[112:115]\n"
    "v_cvt_pk_bf16_f32 v158, v108, v109\n"
    "v_mfma_f32_16x16x32_bf16 v[112:115], a[112:115], v[0:3], v[112:115]\n"
    "buffer_store_dword v162, v173, s[36:39], s59 offen offset:512 sc1\n"
    "v_mfma_f32_16x16x32_bf16 v[112:115], a[124:127], v[4:7], v[112:115]\n"
    "v_cvt_pk_bf16_f32 v159, v110, v111\n"
    "v_mfma_f32_16x16x32_bf16 v[112:115], a[136:139], v[8:11], v[112:115]\n"
    "ds_write_b32 v169, v158 offset:12800\n"
    "v_mfma_f32_16x16x32_bf16 v[112:115], a[148:151], v[12:15], v[112:115]\n"
    "buffer_store_dword v163, v173, s[36:39], s59 offen offset:768 sc1\n"
    "v_mfma_f32_16x16x32_bf16 v[112:115], a[160:163], v[16:19], v[112:115]\n"
    "ds_write_b32 v169, v159 offset:12804\n"
    "v_mfma_f32_16x16x32_bf16 v[112:115], a[172:175], v[20:23], v[112:115]\n"
    "v_mfma_f32_16x16x32_bf16 v[112:115], a[184:187], v[24:27], v[112:115]\n"
    "buffer_store_dword v164, v173, s[36:39], s59 offen offset:1024 sc1\n"
    "v_mfma_f32_16x16x32_bf16 v[112:115], a[196:199], v[28:31], v[112:115]\n"
    "v_mfma_f32_16x16x32_bf16 v[112:115], a[208:211], v[32:35], v[112:115]\n"
    "v_mfma_f32_16x16x32_bf16 v[112:115], a[220:223], v[36:39], v[112:115]\n"
    "buffer_store_dword v165, v173, s[36:39], s59 offen offset:1280 sc1\n"
    "v_mfma_f32_16x16x32_bf16 v[112:115], a[232:235], v[40:43], v[112:115]\n"
    "v_mfma_f32_16x16x32_bf16 v[112:115], a[244:247], v[44:47], v[112:115]\n"
    "v_mfma_f32_16x16x32_bf16 v[112:115], v[188:191], v[48:51], v[112:115]\n"
    "v_mfma_f32_16x16x32_bf16 v[112:115], v[200:203], v[52:55], v[112:115]\n"
    "v_mfma_f32_16x16x32_bf16 v[112:115], v[212:215], v[56:59], v[112:115]\n"
    "v_mfma_f32_16x16x32_bf16 v[112:115], v[224:227], v[60:63], v[112:115]\n"
    "v_mfma_f32_16x16x32_bf16 v[112:115], v[236:239], v[64:67], v[112:115]\n"
    "s_add_u32 s59, s59, s52\n"
    "v_mfma_f32_16x16x32_bf16 v[112:115], v[248:251], v[68:71], v[112:115]\n"
    "v_mfma_f32_16x16x32_bf16 v[116:119], a[8:11], v[72:75], v[152:155]\n"
    "v_mfma_f32_16x16x32_bf16 v[116:119], a[20:23], v[76:79], v[116:119]\n"
    "ds_read_b128 v[72:75], v166 offset:13056\n"
    "v_mfma_f32_16x16x32_bf16 v[116:119], a[32:35], v[80:83], v[116:119]\n"
    "v_mfma_f32_16x16x32_bf16 v[116:119], a[44:47], v[84:87], v[116:119]\n"
    "ds_read_b128 v[76:79], v166 offset:13120\n"
    "v_mfma_f32_16x16x32_bf16 v[116:119], a[56:59], v[88:91], v[116:119]\n"
    "v_mfma_f32_16x16x32_bf16 v[116:119], a[68:71], v[92:95], v[116:119]\n"
    "ds_read_b128 v[80:83], v166 offset:13184\n"
    "v_mfma_f32_16x16x32_bf16 v[116:119], a[80:83], v[96:99], v[116:119]\n"
    "v_mfma_f32_16x16x32_bf16 v[116:119], a[92:95], v[100:103], v[116:119]\n"
    "ds_read_b128 v[84:87], v167 offset:13056\n"
    "v_mfma_f32_16x16x32_bf16 v[116:119], a[104:107], v[104:107], v[116:119]\n"
    "v_cvt_pk_bf16_f32 v156, v112, v113\n"
    "v_mfma_f32_16x16x32_bf16 v[116:119], a[116:119], v[0:3], v[116:119]\n"
    "v_cvt_pk_bf16_f32 v157, v114, v115\n"
    "v_mfma_f32_16x16x32_bf16 v[116:119], a[128:131], v[4:7], v[116:119]\n"
    "ds_write_b32 v169, v156 offset:12832\n"
    "v_mfma_f32_16x16x32_bf16 v[116:119], a[140:143], v[8:11], v[116:119]\n"
    "ds_write_b32 v169, v157 offset:12836\n"
    "v_mfma_f32_16x16x32_bf16 v[116:119], a[152:155], v[12:15], v[116:119]\n"
    "v_mfma_f32_16x16x32_bf16 v[116:119], a[164:167], v[16:19], v[116:119]\n"
    "ds_read_b128 v[88:91], v167 offset:13120\n"
    "v_mfma_f32_16x16x32_bf16 v[116:119], a[176:179], v[20:23], v[116:119]\n"
    "v_mfma_f32_16x16x32_bf16 v[116:119], a[188:191], v[24:27], v[116:119]\n"
    "ds_read_b128 v[92:95], v167 offset:13184\n"
    "v_mfma_f32_16x16x32_bf16 v[116:119], a[200:203], v[28:31], v[116:119]\n"
    "s_mov_b32 s64, 19584\n"
    "v_mfma_f32_16x16x32_bf16 v[116:119], a[212:215], v[32:35], v[116:119]\n"
    "ds_read_b128 v[96:99], v168 offset:13056\n"
    "v_mfma_f32_16x16x32_bf16 v[116:119], a[224:227], v[36:39], v[116:119]\n"
    "v_mfma_f32_16x16x32_bf16 v[116:119], a[236:239], v[40:43], v[116:119]\n"
    "ds_read_b128 v[100:103], v168 offset:13120\n"
    "v_mfma_f32_16x16x32_bf16 v[116:119], a[248:251], v[44:47], v[116:119]\n"
    "s_add_u32 s61, s61, 1\n"
    "v_mfma_f32_16x16x32_bf16 v[116:119], v[192:195], v[48:51], v[116:119]\n"
    "ds_read_b128 v[104:107], v168 offset:13184\n"
    "v_mfma_f32_16x16x32_bf16 v[116:119], v[204:207], v[52:55], v[116:119]\n"
    "v_mfma_f32_16x16x32_bf16 v[116:119], v[216:219], v[56:59], v[116:119]\n"
    "v_mfma_f32_16x16x32_bf16 v[116:119], v[228:231], v[60:63], v[116:119]\n"
    "s_cmp_lg_u32 s61, 5\n"
    "v_mfma_f32_16x16x32_bf16 v[116:119], v[240:243], v[64:67], v[116:119]\n"
    "v_mfma_f32_16x16x32_bf16 v[116:119], v[252:255], v[68:71], v[116:119]\n"
    "s_cbranch_scc0 .Lconv_epi%=\n"
    ".Lconv_loop%=:\n"
    "s_waitcnt lgkmcnt(9)\n"
    "v_mfma_f32_16x16x32_bf16 v[108:111], a[0:3], v[0:3], v[144:147]\n"
    "s_sub_u32 s55, s47, 73920\n"
    "s_cmp_lt_u32 s55, 28311552\n"
    "s_cselect_b32 s26, s47, 0\n"
    "v_mfma_f32_16x16x32_bf16 v[108:111], a[12:15], v[4:7], v[108:111]\n"
    "s_mov_b32 m0, s49\n"
    "v_mfma_f32_16x16x32_bf16 v[108:111], a[24:27], v[8:11], v[108:111]\n"
    "buffer_load_dwordx4 v174, s[24:27], s48 offen lds\n"
    "v_mfma_f32_16x16x32_bf16 v[108:111], a[36:39], v[12:15], v[108:111]\n"
    "v_mfma_f32_16x16x32_bf16 v[108:111], a[48:51], v[16:19], v[108:111]\n"
    "v_mfma_f32_16x16x32_bf16 v[108:111], a[60:63], v[20:23], v[108:111]\n"
    "s_add_u32 m0, s49, s71\n"
    "v_mfma_f32_16x16x32_bf16 v[108:111], a[72:75], v[24:27], v[108:111]\n"
    "buffer_load_dwordx4 v175, s[24:27], s48 offen lds\n"
    "v_mfma_f32_16x16x32_bf16 v[108:111], a[84:87], v[28:31], v[108:111]\n"
    "v_mfma_f32_16x16x32_bf16 v[108:111], a[96:99], v[32:35], v[108:111]\n"
    "s_waitcnt lgkmcnt(0)\n"
    "v_mfma_f32_16x16x32_bf16 v[108:111], a[108:111], v[36:39], v[108:111]\n"
    "v_cvt_pk_bf16_f32 v156, v116, v117\n"
    "v_mfma_f32_16x16x32_bf16 v[108:111], a[120:123], v[40:43], v[108:111]\n"
    "v_cvt_pk_bf16_f32 v157, v118, v119\n"
    "v_mfma_f32_16x16x32_bf16 v[108:111], a[132:135], v[44:47], v[108:111]\n"
    "ds_write_b32 v169, v156 offset:12864\n"
    "v_mfma_f32_16x16x32_bf16 v[108:111], a[144:147], v[48:51], v[108:111]\n"
    "ds_write_b32 v169, v157 offset:12868\n"
    "v_mfma_f32_16x16x32_bf16 v[108:111], a[156:159], v[52:55], v[108:111]\n"
    "v_mfma_f32_16x16x32_bf16 v[108:111], a[168:171], v[56:59], v[108:111]\n"
    "v_add_u32 v166, s64, v166\n"
    "v_mfma_f32_16x16x32_bf16 v[108:111], a[180:183], v[60:63], v[108:111]\n"
    "v_add_u32 v167, s64, v167\n"
    "v_mfma_f32_16x16x32_bf16 v[108:111], a[192:195], v[64:67], v[108:111]\n"
    "v_add_u32 v168, s64, v168\n"
    "v_mfma_f32_16x16x32_bf16 v[108:111], a[204:207], v[68:71], v[108:111]\n"
    "s_waitcnt lgkmcnt(0)\n"
    "v_mfma_f32_16x16x32_bf16 v[108:111], a[216:219], v[72:75], v[108:111]\n"
    "s_waitcnt vmcnt(24)\n"
    "v_mfma_f32_16x16x32_bf16 v[108:111], a[228:231], v[76:79], v[108:111]\n"
    "s_barrier\n"
    "v_mfma_f32_16x16x32_bf16 v[108:111], a[240:243], v[80:83], v[108:111]\n"
    "ds_read_b32 v160, v170 offset:12800\n"
    "v_mfma_f32_16x16x32_bf16 v[108:111], a[252:255], v[84:87], v[108:111]\n"
    "ds_read_b32 v161, v171 offset:12800\n"
    "v_mfma_f32_16x16x32_bf16 v[108:111], v[196:199], v[88:91], v[108:111]\n"
    "ds_read_b32 v162, v172 offset:12800\n"
    "v_mfma_f32_16x16x32_bf16 v[108:111], v[208:211], v[92:95], v[108:111]\n"
    "ds_read_b32 v163, v170 offset:13600\n"
    "v_mfma_f32_16x16x32_bf16 v[108:111], v[220:223], v[96:99], v[108:111]\n"
    "ds_read_b32 v164, v171 offset:13600\n"
    "v_mfma_f32_16x16x32_bf16 v[108:111], v[232:235], v[100:103], v[108:111]\n"
    "ds_read_b32 v165, v172 offset:13600\n"
    "v_mfma_f32_16x16x32_bf16 v[108:111], v[244:247], v[104:107], v[108:111]\n"
    "s_sub_u32 s44, s44, 1\n"
    "v_mfma_f32_16x16x32_bf16 v[112:115], a[4:7], v[0:3], v[148:151]\n"
    "s_cmp_lg_u32 s44, 0\n"
    "s_cselect_b32 s55, s52, s53\n"
    "s_cselect_b32 s56, s52, s54\n"
    "v_mfma_f32_16x16x32_bf16 v[112:115], a[16:19], v[4:7], v[112:115]\n"
    "s_cmp_lg_u32 s44, 0\n"
    "s_cselect_b32 s44, s44, 7\n"
    "v_mfma_f32_16x16x32_bf16 v[112:115], a[28:31], v[8:11], v[112:115]\n"
    "s_waitcnt lgkmcnt(0)\n"
    "v_mfma_f32_16x16x32_bf16 v[112:115], a[40:43], v[12:15], v[112:115]\n"
    "buffer_store_dword v160, v173, s[36:39], s59 offen offset:0 sc1\n"
    "v_mfma_f32_16x16x32_bf16 v[112:115], a[52:55], v[16:19], v[112:115]\n"
    "s_add_u32 s47, s47, s55\n"
    "s_add_u32 s48, s48, s56\n"
    "v_mfma_f32_16x16x32_bf16 v[112:115], a[64:67], v[20:23], v[112:115]\n"
    "s_add_u32 s49, s49, 6528\n"
    "s_min_u32 s49, s49, s50\n"
    "v_mfma_f32_16x16x32_bf16 v[112:115], a[76:79], v[24:27], v[112:115]\n"
    "buffer_store_dword v161, v173, s[36:39], s59 offen offset:256 sc1\n"
    "v_mfma_f32_16x16x32_bf16 v[112:115], a[88:91], v[28:31], v[112:115]\n"
    "v_mfma_f32_16x16x32_bf16 v[112:115], a[100:103], v[32:35], v[112:115]\n"
    "v_cvt_pk_bf16_f32 v158, v108, v109\n"
    "v_mfma_f32_16x16x32_bf16 v[112:115], a[112:115], v[36:39], v[112:115]\n"
    "buffer_store_dword v162, v173, s[36:39], s59 offen offset:512 sc1\n"
    "v_mfma_f32_16x16x32_bf16 v[112:115], a[124:127], v[40:43], v[112:115]\n"
    "v_cvt_pk_bf16_f32 v159, v110, v111\n"
    "v_mfma_f32_16x16x32_bf16 v[112:115], a[136:139], v[44:47], v[112:115]\n"
    "ds_write_b32 v169, v158 offset:0\n"
    "v_mfma_f32_16x16x32_bf16 v[112:115], a[148:151], v[48:51], v[112:115]\n"
    "buffer_store_dword v163, v173, s[36:39], s59 offen offset:768 sc1\n"
    "v_mfma_f32_16x16x32_bf16 v[112:115], a[160:163], v[52:55], v[112:115]\n"
    "ds_write_b32 v169, v159 offset:4\n"
    "v_mfma_f32_16x16x32_bf16 v[112:115], a[172:175], v[56:59], v[112:115]\n"
    "v_mfma_f32_16x16x32_bf16 v[112:115], a[184:187], v[60:63], v[112:115]\n"
    "buffer_store_dword v164, v173, s[36:39], s59 offen offset:1024 sc1\n"
    "v_mfma_f32_16x16x32_bf16 v[112:115], a[196:199], v[64:67], v[112:115]\n"
    "v_mfma_f32_16x16x32_bf16 v[112:115], a[208:211], v[68:71], v[112:115]\n"
    "v_mfma_f32_16x16x32_bf16 v[112:115], a[220:223], v[72:75], v[112:115]\n"
    "buffer_store_dword v165, v173, s[36:39], s59 offen offset:1280 sc1\n"
    "v_mfma_f32_16x16x32_bf16 v[112:115], a[232:235], v[76:79], v[112:115]\n"
    "v_mfma_f32_16x16x32_bf16 v[112:115], a[244:247], v[80:83], v[112:115]\n"
    "v_mfma_f32_16x16x32_bf16 v[112:115], v[188:191], v[84:87], v[112:115]\n"
    "v_mfma_f32_16x16x32_bf16 v[112:115], v[200:203], v[88:91], v[112:115]\n"
    "v_mfma_f32_16x16x32_bf16 v[112:115], v[212:215], v[92:95], v[112:115]\n"
    "v_mfma_f32_16x16x32_bf16 v[112:115], v[224:227], v[96:99], v[112:115]\n"
    "v_mfma_f32_16x16x32_bf16 v[112:115], v[236:239], v[100:103], v[112:115]\n"
    "s_add_u32 s59, s59, s60\n"
    "s_mov_b32 s60, s52\n"
    "v_mfma_f32_16x16x32_bf16 v[112:115], v[248:251], v[104:107], v[112:115]\n"
    "v_mfma_f32_16x16x32_bf16 v[116:119], a[8:11], v[0:3], v[152:155]\n"
    "v_mfma_f32_16x16x32_bf16 v[116:119], a[20:23], v[4:7], v[116:119]\n"
    "ds_read_b128 v[0:3], v166 offset:0\n"
    "v_mfma_f32_16x16x32_bf16 v[116:119], a[32:35], v[8:11], v[116:119]\n"
    "v_mfma_f32_16x16x32_bf16 v[116:119], a[44:47], v[12:15], v[116:119]\n"
    "ds_read_b128 v[4:7], v166 offset:64\n"
    "v_mfma_f32_16x16x32_bf16 v[116:119], a[56:59], v[16:19], v[116:119]\n"
    "v_mfma_f32_16x16x32_bf16 v[116:119], a[68:71], v[20:23], v[116:119]\n"
    "ds_read_b128 v[8:11], v166 offset:128\n"
    "v_mfma_f32_16x16x32_bf16 v[116:119], a[80:83], v[24:27], v[116:119]\n"
    "v_mfma_f32_16x16x32_bf16 v[116:119], a[92:95], v[28:31], v[116:119]\n"
    "ds_read_b128 v[12:15], v167 offset:0\n"
    "v_mfma_f32_16x16x32_bf16 v[116:119], a[104:107], v[32:35], v[116:119]\n"
    "v_cvt_pk_bf16_f32 v156, v112, v113\n"
    "v_mfma_f32_16x16x32_bf16 v[116:119], a[116:119], v[36:39], v[116:119]\n"
    "v_cvt_pk_bf16_f32 v157, v114, v115\n"
    "v_mfma_f32_16x16x32_bf16 v[116:119], a[128:131], v[40:43], v[116:119]\n"
    "ds_write_b32 v169, v156 offset:32\n"
    "v_mfma_f32_16x16x32_bf16 v[116:119], a[140:143], v[44:47], v[116:119]\n"
    "ds_write_b32 v169, v157 offset:36\n"
    "v_mfma_f32_16x16x32_bf16 v[116:119], a[152:155], v[48:51], v[116:119]\n"
    "v_mfma_f32_16x16x32_bf16 v[116:119], a[164:167], v[52:55], v[116:119]\n"
    "ds_read_b128 v[16:19], v167 offset:64\n"
    "v_mfma_f32_16x16x32_bf16 v[116:119], a[176:179], v[56:59], v[116:119]\n"
    "v_mfma_f32_16x16x32_bf16 v[116:119], a[188:191], v[60:63], v[116:119]\n"
    "ds_read_b128 v[20:23], v167 offset:128\n"
    "v_mfma_f32_16x16x32_bf16 v[116:119], a[200:203], v[64:67], v[116:119]\n"
    "v_mfma_f32_16x16x32_bf16 v[116:119], a[212:215], v[68:71], v[116:119]\n"
    "ds_read_b128 v[24:27], v168 offset:0\n"
    "v_mfma_f32_16x16x32_bf16 v[116:119], a[224:227], v[72:75], v[116:119]\n"
    "v_mfma_f32_16x16x32_bf16 v[116:119], a[236:239], v[76:79], v[116:119]\n"
    "ds_read_b128 v[28:31], v168 offset:64\n"
    "v_mfma_f32_16x16x32_bf16 v[116:119], a[248:251], v[80:83], v[116:119]\n"
    "v_mfma_f32_16x16x32_bf16 v[116:119], v[192:195], v[84:87], v[116:119]\n"
    "ds_read_b128 v[32:35], v168 offset:128\n"
    "v_mfma_f32_16x16x32_bf16 v[116:119], v[204:207], v[88:91], v[116:119]\n"
    "v_mfma_f32_16x16x32_bf16 v[116:119], v[216:219], v[92:95], v[116:119]\n"
    "v_mfma_f32_16x16x32_bf16 v[116:119], v[228:231], v[96:99], v[116:119]\n"
    "v_mfma_f32_16x16x32_bf16 v[116:119], v[240:243], v[100:103], v[116:119]\n"
    "v_mfma_f32_16x16x32_bf16 v[116:119], v[252:255], v[104:107], v[116:119]\n"
    "s_branch .Lconv_r1%=\n"
    ".Lconv_pad3%=:\n"
    "s_branch .Lconv_back%=\n"
    ".Lconv_epi%=:\n"
    "s_nop 7\n"
    "v_cvt_pk_bf16_f32 v156, v116, v117\n"
    "v_cvt_pk_bf16_f32 v157, v118, v119\n"
    "ds_write_b32 v169, v156 offset:12864\n"
    "ds_write_b32 v169, v157 offset:12868\n"
    "s_waitcnt lgkmcnt(0)\n"
    "s_barrier\n"
    "ds_read_b32 v160, v170 offset:12800\n"
    "ds_read_b32 v161, v171 offset:12800\n"
    "ds_read_b32 v162, v172 offset:12800\n"
    "ds_read_b32 v163, v170 offset:13600\n"
    "ds_read_b32 v164, v171 offset:13600\n"
    "ds_read_b32 v165, v172 offset:13600\n"
    "s_waitcnt lgkmcnt(5)\n"
    "buffer_store_dword v160, v173, s[36:39], s59 offen offset:0 sc1\n"
    "s_waitcnt lgkmcnt(4)\n"
    "buffer_store_dword v161, v173, s[36:39], s59 offen offset:256 sc1\n"
    "s_waitcnt lgkmcnt(3)\n"
    "buffer_store_dword v162, v173, s[36:39], s59 offen offset:512 sc1\n"
    "s_waitcnt lgkmcnt(2)\n"
    "buffer_store_dword v163, v173, s[36:39], s59 offen offset:768 sc1\n"
    "s_waitcnt lgkmcnt(1)\n"
    "buffer_store_dword v164, v173, s[36:39], s59 offen offset:1024 sc1\n"
    "s_waitcnt lgkmcnt(0)\n"
    "buffer_store_dword v165, v173, s[36:39], s59 offen offset:1280 sc1\n"
    "s_endpgm\n"
    ".Lconv_pad2%=:\n"
    "s_branch .Lconv_back%=\n"
    ".Lconv_extra%=:\n"
    "s_mov_b32 s26, s47\n"
    "s_mov_b32 m0, s49\n"
    "s_nop 0\n"
    "buffer_load_dwordx4 v174, s[24:27], s48 offen lds\n"
    "s_add_u32 m0, s49, s71\n"
    "s_nop 0\n"
    "buffer_load_dwordx4 v175, s[24:27], s48 offen lds\n"
    "s_add_u32 s47, s47, s52\n"
    "s_add_u32 s48, s48, s52\n"
    "s_add_u32 s49, s49, 6528\n"
    "s_branch .Lconv_extraret%=\n"
    ".Lconv_switch%=:\n"
    "v_mfma_f32_16x16x32_bf16 v[108:111], a[0:3], v[72:75], v[144:147]\n"
    "s_sub_u32 s55, s47, 73920\n"
    "s_cmp_lt_u32 s55, 28311552\n"
    "s_cselect_b32 s26, s47, 0\n"
    "v_mfma_f32_16x16x32_bf16 v[108:111], a[12:15], v[76:79], v[108:111]\n"
    "s_mov_b32 m0, s49\n"
    "v_mfma_f32_16x16x32_bf16 v[108:111], a[24:27], v[80:83], v[108:111]\n"
    "buffer_load_dwordx4 v174, s[24:27], s48 offen lds\n"
    "v_mfma_f32_16x16x32_bf16 v[108:111], a[36:39], v[84:87], v[108:111]\n"
    "s_add_u32 m0, s49, s71\n"
    "v_mfma_f32_16x16x32_bf16 v[108:111], a[48:51], v[88:91], v[108:111]\n"
    "buffer_load_dwordx4 v175, s[24:27], s48 offen lds\n"
    "v_mfma_f32_16x16x32_bf16 v[108:111], a[60:63], v[92:95], v[108:111]\n"
    "s_sub_u32 s44, s44, 1\n"
    "s_cmp_lg_u32 s44, 0\n"
    "s_cselect_b32 s55, s52, s53\n"
    "s_cselect_b32 s56, s52, s54\n"
    "v_mfma_f32_16x16x32_bf16 v[108:111], a[72:75], v[96:99], v[108:111]\n"
    "s_cmp_lg_u32 s44, 0\n"
    "s_cselect_b32 s44, s44, 7\n"
    "s_add_u32 s47, s47, s55\n"
    "s_add_u32 s48, s48, s56\n"
    "v_mfma_f32_16x16x32_bf16 v[108:111], a[84:87], v[100:103], v[108:111]\n"
    "s_add_u32 s49, s49, 6528\n"
    "s_min_u32 s49, s49, s50\n"
    "v_mfma_f32_16x16x32_bf16 v[108:111], a[96:99], v[104:107], v[108:111]\n"
    "s_waitcnt lgkmcnt(0)\n"
    "v_cvt_pk_bf16_f32 v156, v116, v117\n"
    "v_mfma_f32_16x16x32_bf16 v[108:111], a[108:111], v[0:3], v[108:111]\n"
    "v_cvt_pk_bf16_f32 v157, v118, v119\n"
    "v_mfma_f32_16x16x32_bf16 v[108:111], a[120:123], v[4:7], v[108:111]\n"
    "ds_write_b32 v169, v156 offset:6464\n"
    "v_mfma_f32_16x16x32_bf16 v[108:111], a[132:135], v[8:11], v[108:111]\n"
    "ds_write_b32 v169, v157 offset:6468\n"
    "v_mfma_f32_16x16x32_bf16 v[108:111], a[144:147], v[12:15], v[108:111]\n"
    "s_sub_u32 s55, s47, 73920\n"
    "s_cmp_lt_u32 s55, 28311552\n"
    "s_cselect_b32 s26, s47, 0\n"
    "v_mfma_f32_16x16x32_bf16 v[108:111], a[156:159], v[16:19], v[108:111]\n"
    "s_mov_b32 s64, 26112\n"
    "s_mov_b32 s60, s99\n"
    "v_mfma_f32_16x16x32_bf16 v[108:111], a[168:171], v[20:23], v[108:111]\n"
    "v_mfma_f32_16x16x32_bf16 v[108:111], a[180:183], v[24:27], v[108:111]\n"
    "s_waitcnt lgkmcnt(0)\n"
    "v_mfma_f32_16x16x32_bf16 v[108:111], a[192:195], v[28:31], v[108:111]\n"
    "s_waitcnt vmcnt(16)\n"
    "v_mfma_f32_16x16x32_bf16 v[108:111], a[204:207], v[32:35], v[108:111]\n"
    "s_barrier\n"
    "v_mfma_f32_16x16x32_bf16 v[112:115], a[4:7], v[72:75], v[148:151]\n"
    "ds_read_b32 v160, v170 offset:6400\n"
    "v_mfma_f32_16x16x32_bf16 v[112:115], a[16:19], v[76:79], v[112:115]\n"
    "ds_read_b32 v161, v171 offset:6400\n"
    "v_mfma_f32_16x16x32_bf16 v[112:115], a[28:31], v[80:83], v[112:115]\n"
    "ds_read_b32 v162, v172 offset:6400\n"
    "v_mfma_f32_16x16x32_bf16 v[112:115], a[40:43], v[84:87], v[112:115]\n"
    "ds_read_b32 v163, v170 offset:7200\n"
    "v_mfma_f32_16x16x32_bf16 v[112:115], a[52:55], v[88:91], v[112:115]\n"
    "ds_read_b32 v164, v171 offset:7200\n"
    "v_mfma_f32_16x16x32_bf16 v[112:115], a[64:67], v[92:95], v[112:115]\n"
    "ds_read_b32 v165, v172 offset:7200\n"
    "v_mfma_f32_16x16x32_bf16 v[112:115], a[76:79], v[96:99], v[112:115]\n"
    "ds_read_b128 v[36:39], v166 offset:13056\n"
    "v_mfma_f32_16x16x32_bf16 v[112:115], a[88:91], v[100:103], v[112:115]\n"
    "ds_read_b128 v[40:43], v166 offset:13120\n"
    "v_mfma_f32_16x16x32_bf16 v[112:115], a[100:103], v[104:107], v[112:115]\n"
    "ds_read_b128 v[44:47], v166 offset:13184\n"
    "v_cvt_pk_bf16_f32 v158, v108, v109\n"
    "v_mfma_f32_16x16x32_bf16 v[112:115], a[112:115], v[0:3], v[112:115]\n"
    "ds_read_b128 v[48:51], v167 offset:13056\n"
    "v_cvt_pk_bf16_f32 v159, v110, v111\n"
    "v_mfma_f32_16x16x32_bf16 v[112:115], a[124:127], v[4:7], v[112:115]\n"
    "ds_read_b128 v[52:55], v167 offset:13120\n"
    "v_mfma_f32_16x16x32_bf16 v[112:115], a[136:139], v[8:11], v[112:115]\n"
    "s_waitcnt lgkmcnt(5)\n"
    "buffer_store_dword v160, v173, s[36:39], s59 offen offset:0 sc1\n"
    "ds_read_b128 v[56:59], v167 offset:13184\n"
    "v_mfma_f32_16x16x32_bf16 v[112:115], a[148:151], v[12:15], v[112:115]\n"
    "ds_read_b128 v[60:63], v168 offset:13056\n"
    "v_mfma_f32_16x16x32_bf16 v[112:115], a[160:163], v[16:19], v[112:115]\n"
    "buffer_store_dword v161, v173, s[36:39], s59 offen offset:256 sc1\n"
    "ds_read_b128 v[64:67], v168 offset:13120\n"
    "v_mfma_f32_16x16x32_bf16 v[112:115], a[172:175], v[20:23], v[112:115]\n"
    "ds_read_b128 v[68:71], v168 offset:13184\n"
    "v_mfma_f32_16x16x32_bf16 v[112:115], a[184:187], v[24:27], v[112:115]\n"
    "ds_write_b32 v169, v158 offset:12800\n"
    "v_mfma_f32_16x16x32_bf16 v[112:115], a[196:199], v[28:31], v[112:115]\n"
    "ds_write_b32 v169, v159 offset:12804\n"
    "v_mfma_f32_16x16x32_bf16 v[112:115], a[208:211], v[32:35], v[112:115]\n"
    "buffer_store_dword v162, v173, s[36:39], s59 offen offset:512 sc1\n"
    "v_mfma_f32_16x16x32_bf16 v[116:119], a[116:119], v[0:3], v[152:155]\n"
    "ds_read_b128 v[0:3], v166 offset:6528\n"
    "v_mfma_f32_16x16x32_bf16 v[116:119], a[128:131], v[4:7], v[116:119]\n"
    "buffer_store_dword v163, v173, s[36:39], s59 offen offset:768 sc1\n"
    "ds_read_b128 v[4:7], v166 offset:6592\n"
    "v_mfma_f32_16x16x32_bf16 v[116:119], a[140:143], v[8:11], v[116:119]\n"
    "ds_read_b128 v[8:11], v166 offset:6656\n"
    "v_mfma_f32_16x16x32_bf16 v[116:119], a[152:155], v[12:15], v[116:119]\n"
    "buffer_store_dword v164, v173, s[36:39], s59 offen offset:1024 sc1\n"
    "ds_read_b128 v[12:15], v167 offset:6528\n"
    "v_mfma_f32_16x16x32_bf16 v[116:119], a[164:167], v[16:19], v[116:119]\n"
    "ds_read_b128 v[16:19], v167 offset:6592\n"
    "v_mfma_f32_16x16x32_bf16 v[116:119], a[176:179], v[20:23], v[116:119]\n"
    "buffer_store_dword v165, v173, s[36:39], s59 offen offset:1280 sc1\n"
    "ds_read_b128 v[20:23], v167 offset:6656\n"
    "v_mfma_f32_16x16x32_bf16 v[116:119], a[188:191], v[24:27], v[116:119]\n"
    "ds_read_b128 v[24:27], v168 offset:6528\n"
    "s_add_u32 s59, s59, s52\n"
    "s_mov_b32 m0, s49\n"
    "v_mfma_f32_16x16x32_bf16 v[116:119], a[200:203], v[28:31], v[116:119]\n"
    "ds_read_b128 v[28:31], v168 offset:6592\n"
    "v_cvt_pk_bf16_f32 v156, v112, v113\n"
    "v_mfma_f32_16x16x32_bf16 v[116:119], a[212:215], v[32:35], v[116:119]\n"
    "ds_read_b128 v[32:35], v168 offset:6656\n"
    "v_cvt_pk_bf16_f32 v157, v114, v115\n"
    "ds_write_b32 v169, v156 offset:12832\n"
    "ds_write_b32 v169, v157 offset:12836\n"
    "v_mfma_f32_16x16x32_bf16 v[116:119], a[8:11], v[72:75], v[116:119]\n"
    "buffer_load_dwordx4 v174, s[24:27], s48 offen lds\n"
    "ds_read_b128 v[72:75], v166 offset:19584\n"
    "v_mfma_f32_16x16x32_bf16 v[116:119], a[20:23], v[76:79], v[116:119]\n"
    "ds_read_b128 v[76:79], v166 offset:19648\n"
    "s_add_u32 m0, s49, s71\n"
    "v_mfma_f32_16x16x32_bf16 v[116:119], a[32:35], v[80:83], v[116:119]\n"
    "buffer_load_dwordx4 v175, s[24:27], s48 offen lds\n"
    "ds_read_b128 v[80:83], v166 offset:19712\n"
    "v_mfma_f32_16x16x32_bf16 v[116:119], a[44:47], v[84:87], v[116:119]\n"
    "ds_read_b128 v[84:87], v167 offset:19584\n"
    "s_sub_u32 s44, s44, 1\n"
    "s_cmp_lg_u32 s44, 0\n"
    "s_cselect_b32 s55, s52, s53\n"
    "s_cselect_b32 s56, s52, s54\n"
    "v_mfma_f32_16x16x32_bf16 v[116:119], a[56:59], v[88:91], v[116:119]\n"
    "ds_read_b128 v[88:91], v167 offset:19648\n"
    "s_cmp_lg_u32 s44, 0\n"
    "s_cselect_b32 s44, s44, 7\n"
    "s_add_u32 s47, s47, s55\n"
    "s_add_u32 s48, s48, s56\n"
    "v_mfma_f32_16x16x32_bf16 v[116:119], a[68:71], v[92:95], v[116:119]\n"
    "ds_read_b128 v[92:95], v167 offset:19712\n"
    "s_add_u32 s49, s49, 6528\n"
    "s_min_u32 s49, s49, s50\n"
    "v_mfma_f32_16x16x32_bf16 v[116:119], a[80:83], v[96:99], v[116:119]\n"
    "ds_read_b128 v[96:99], v168 offset:19584\n"
    "s_add_u32 s61, s61, 1\n"
    "v_mfma_f32_16x16x32_bf16 v[116:119], a[92:95], v[100:103], v[116:119]\n"
    "ds_read_b128 v[100:103], v168 offset:19648\n"
    "s_cmp_lg_u32 s61, 5\n"
    "v_mfma_f32_16x16x32_bf16 v[116:119], a[104:107], v[104:107], v[116:119]\n"
    "ds_read_b128 v[104:107], v168 offset:19712\n"
    "s_cbranch_scc1 .Lconv_loop%=\n"
    "s_branch .Lconv_epi%=\n"
    ".p2align 12\n"
    :
    : "s"(__builtin_amdgcn_kernarg_segment_ptr()), "s"(blockIdx.x), "s"(__builtin_amdgcn_readfirstlane(threadIdx.x >> 6)), "s"((unsigned)(size_t)lds)
    : "v0", "v1", "v2", "v3", "v4", "v5", "v6", "v7", "v8", "v9", "v10", "v11", "v12", "v13", "v14", "v15", "v16", "v17", "v18", "v19", "v20", "v21", "v22", "v23", "v24", "v25", "v26", "v27", "v28", "v29", "v30", "v31", "v32", "v33", "v34", "v35", "v36", "v37", "v38", "v39", "v40", "v41", "v42", "v43", "v44", "v45", "v46", "v47", "v48", "v49", "v50", "v51", "v52", "v53", "v54", "v55", "v56", "v57", "v58", "v59", "v60", "v61", "v62", "v63", "v64", "v65", "v66", "v67", "v68", "v69", "v70", "v71", "v72", "v73", "v74", "v75", "v76", "v77", "v78", "v79", "v80", "v81", "v82", "v83", "v84", "v85", "v86", "v87", "v88", "v89", "v90", "v91", "v92", "v93", "v94", "v95", "v96", "v97", "v98", "v99", "v100", "v101", "v102", "v103", "v104", "v105", "v106", "v107", "v108", "v109", "v110", "v111", "v112", "v113", "v114", "v115", "v116", "v117", "v118", "v119", "v120", "v121", "v122", "v123", "v124", "v125", "v126", "v127", "v128", "v129", "v130", "v131", "v132", "v133", "v134", "v135", "v136", "v137", "v138", "v139", "v140", "v141", "v142", "v143", "v144", "v145", "v146", "v147", "v148", "v149", "v150", "v151", "v152", "v153", "v154", "v155", "v156", "v157", "v158", "v159", "v160", "v161", "v162", "v163", "v164", "v165", "v166", "v167", "v168", "v169", "v170", "v171", "v172", "v173", "v174", "v175", "v176", "v177", "v178", "v179", "v180", "v181", "v182", "v183", "v184", "v185", "v186", "v187", "v188", "v189", "v190", "v191", "v192", "v193", "v194", "v195", "v196", "v197", "v198", "v199", "v200", "v201", "v202", "v203", "v204", "v205", "v206", "v207", "v208", "v209", "v210", "v211", "v212", "v213", "v214", "v215", "v216", "v217", "v218", "v219", "v220", "v221", "v222", "v223", "v224", "v225", "v226", "v227", "v228", "v229", "v230", "v231", "v232", "v233", "v234", "v235", "v236", "v237", "v238", "v239", "v240", "v241", "v242", "v243", "v244", "v245", "v246", "v247", "v248", "v249", "v250", "v251", "v252", "v253", "v254", "v255", "a0", "a1", "a2", "a3", "a4", "a5", "a6", "a7", "a8", "a9", "a10", "a11", "a12", "a13", "a14", "a15", "a16", "a17", "a18", "a19", "a20", "a21", "a22", "a23", "a24", "a25", "a26", "a27", "a28", "a29", "a30", "a31", "a32", "a33", "a34", "a35", "a36", "a37", "a38", "a39", "a40", "a41", "a42", "a43", "a44", "a45", "a46", "a47", "a48", "a49", "a50", "a51", "a52", "a53", "a54", "a55", "a56", "a57", "a58", "a59", "a60", "a61", "a62", "a63", "a64", "a65", "a66", "a67", "a68", "a69", "a70", "a71", "a72", "a73", "a74", "a75", "a76", "a77", "a78", "a79", "a80", "a81", "a82", "a83", "a84", "a85", "a86", "a87", "a88", "a89", "a90", "a91", "a92", "a93", "a94", "a95", "a96", "a97", "a98", "a99", "a100", "a101", "a102", "a103", "a104", "a105", "a106", "a107", "a108", "a109", "a110", "a111", "a112", "a113", "a114", "a115", "a116", "a117", "a118", "a119", "a120", "a121", "a122", "a123", "a124", "a125", "a126", "a127", "a128", "a129", "a130", "a131", "a132", "a133", "a134", "a135", "a136", "a137", "a138", "a139", "a140", "a141", "a142", "a143", "a144", "a145", "a146", "a147", "a148", "a149", "a150", "a151", "a152", "a153", "a154", "a155", "a156", "a157", "a158", "a159", "a160", "a161", "a162", "a163", "a164", "a165", "a166", "a167", "a168", "a169", "a170", "a171", "a172", "a173", "a174", "a175", "a176", "a177", "a178", "a179", "a180", "a181", "a182", "a183", "a184", "a185", "a186", "a187", "a188", "a189", "a190", "a191", "a192", "a193", "a194", "a195", "a196", "a197", "a198", "a199", "a200", "a201", "a202", "a203", "a204", "a205", "a206", "a207", "a208", "a209", "a210", "a211", "a212", "a213", "a214", "a215", "a216", "a217", "a218", "a219", "a220", "a221", "a222", "a223", "a224", "a225", "a226", "a227", "a228", "a229", "a230", "a231", "a232", "a233", "a234", "a235", "a236", "a237", "a238", "a239", "a240", "a241", "a242", "a243", "a244", "a245", "a246", "a247", "a248", "a249", "a250", "a251", "a252", "a253", "a254", "a255", "s16", "s17", "s18", "s19", "s20", "s21", "s22", "s23", "s24", "s25", "s26", "s27", "s28", "s29", "s30", "s31", "s36", "s37", "s38", "s39", "s40", "s41", "s42", "s43", "s44", "s45", "s46", "s47", "s48", "s49", "s50", "s51", "s52", "s53", "s54", "s55", "s56", "s57", "s58", "s59", "s60", "s61", "s62", "s63", "s64", "s65", "s66", "s67", "s68", "s69", "s70", "s71", "s72", "s73", "s74", "s75", "s76", "s77", "s78", "s79", "s80", "s81", "s82", "s83", "s84", "s85", "s86", "s87", "s88", "s89", "s90", "s91", "s92", "s93", "s94", "s95", "s96", "s97", "s98", "s99", "vcc", "scc", "memory");
}

extern "C" void kernel_launch(void* const* d_in, const int* in_sizes, int n_in, void* d_out, int out_size, void* d_ws, size_t ws_size, hipStream_t stream) {
  conv3x3_kernel<<<256, 256, 0, stream>>>(d_in[0], d_in[1], d_in[2], d_out);
}
```
